```python
import torch

def _softmax(x, axis=-1):
    return torch.softmax(x, dim=axis)
def _gelu(x):
    return torch.nn.functional.gelu(x, approximate="tanh")
def _silu(x):
    return torch.nn.functional.silu(x)
def _sigmoid(x):
    return torch.sigmoid(x)
def _layernorm(x, axis=-1, eps=1e-5):
    m = x.mean(dim=axis, keepdim=True)
    v = x.var(dim=axis, keepdim=True, unbiased=False)
    return (x - m) / torch.sqrt(v + eps)
def _scatter_add(tgt, idx, src, axis=0):
    return tgt.clone().index_add_(axis, idx, src)
def _segment_sum(src, seg, n):
    out = torch.zeros((n,) + src.shape[1:], dtype=src.dtype)
    return out.index_add_(0, seg, src)
def _pad(x, pw):
    flat = tuple(v for pair in reversed(pw) for v in pair)
    return torch.nn.functional.pad(x, flat)

kernel_name = 'parallel_sum__spectral_gfnet__glu_gated_geglu__prompt'

def setup_inputs():

    _w = (lambda a: (lambda t: t.to(torch.bfloat16) if t.is_floating_point() else t)(torch.as_tensor(a)))
    def _A_setup():
        B, S, D = 1, 2048, 512
        Df = D // 2 + 1
        return {
            "x": _w((lambda *s: torch.randn(*s, dtype=torch.bfloat16) * 0.05)(B, S, D)),
            "w_r": _w((lambda *s: torch.randn(*s, dtype=torch.bfloat16) * 0.8)(S, Df) * 0.02),
            "w_i": _w((lambda *s: torch.randn(*s, dtype=torch.bfloat16) * 0.8)(S, Df) * 0.02),
        }
    _A_in = _A_setup()

    def _B_setup():
        B, S, Dm, Df = 1, 2048, 512, 1792
        return {
            "x": _w((lambda *s: torch.randn(*s, dtype=torch.bfloat16) * 0.05)(B, S, Dm)),
            "w_gate": _w((lambda *s: torch.randn(*s, dtype=torch.bfloat16) * 0.4)(Dm, Df) * (Dm ** -0.5)),
            "w_up": _w((lambda *s: torch.randn(*s, dtype=torch.bfloat16) * 0.4)(Dm, Df) * (Dm ** -0.5)),
            "w_down": _w((lambda *s: torch.randn(*s, dtype=torch.bfloat16) * 0.4)(Df, Dm) * (Df ** -0.5)),
        }
    _B_in = _B_setup()

    return {
        "A_x": _A_in["x"],
        "A_w_r": _A_in["w_r"],
        "A_w_i": _A_in["w_i"],
        "B_x": _B_in["x"],
        "B_w_gate": _B_in["w_gate"],
        "B_w_up": _B_in["w_up"],
        "B_w_down": _B_in["w_down"],
    }

def reference(A_x, A_w_r, A_w_i, B_x, B_w_gate, B_w_up, B_w_down):
    def _A_ref(x, w_r, w_i):
        B, S, D = x.shape
        X = (lambda x, axes=(-2,-1): torch.fft.rfft2(x.float(), dim=axes))(x, axes=(-2, -1))
        W = w_r + 1j * w_i
        X = X * W
        y = (lambda x, s, axes=(-2,-1): torch.fft.irfft2(x, s=s, dim=axes).to(torch.bfloat16))(X, (S, D), axes=(-2, -1))
        return y

    def _B_ref(x, w_gate, w_up, w_down):
        g = torch.einsum("bsd,df->bsf", x, w_gate)
        u = torch.einsum("bsd,df->bsf", x, w_up)
        h = _gelu(g) * u
        return torch.einsum("bsf,fd->bsd", h, w_down)

    x = A_x
    xn = _layernorm(x, axis=-1)
    return x + _A_ref(xn, A_w_r, A_w_i) + _B_ref(xn, B_w_gate, B_w_up, B_w_down)
```

```cpp
#include <hip/hip_runtime.h>
#include <hip/hip_bf16.h>
#pragma clang diagnostic ignored "-Wint-to-pointer-cast"

typedef __bf16 bf16x8 __attribute__((ext_vector_type(8)));
typedef __bf16 bf16x2 __attribute__((ext_vector_type(2)));
typedef short s16x4 __attribute__((ext_vector_type(4)));
typedef short s16x8 __attribute__((ext_vector_type(8)));
typedef float f32x16 __attribute__((ext_vector_type(16)));
typedef float f32x4 __attribute__((ext_vector_type(4)));
typedef float f32x2 __attribute__((ext_vector_type(2)));
typedef unsigned u32x4 __attribute__((ext_vector_type(4)));
typedef unsigned u32x2 __attribute__((ext_vector_type(2)));
#define LDSP __attribute__((address_space(3)))
#define DI __device__ __forceinline__
typedef __amdgpu_buffer_rsrc_t rsrc_t;

constexpr unsigned X_BYTES = 2048u * 512u * 2u;
constexpr unsigned W1_BYTES = 512u * 1792u * 2u;
constexpr unsigned WF_BYTES = 2048u * 257u * 2u;
constexpr unsigned H_BYTES = 2048u * 1792u * 2u;
constexpr unsigned T_BYTES = 256u * 8192u;
constexpr unsigned WT_BYTES = 257u * 8192u;
constexpr unsigned OFF_H = 0, OFF_T = H_BYTES, OFF_Z = OFF_T + T_BYTES, OFF_WT = OFF_Z + T_BYTES;
constexpr int AUX_WT = 16;

DI rsrc_t mk_rsrc(const void* p, unsigned bytes) { return __builtin_amdgcn_make_buffer_rsrc((void*)p, 0, bytes, 0x00020000); }
DI unsigned pk2(float a, float b) { bf16x2 v = {(__bf16)a, (__bf16)b}; return __builtin_bit_cast(unsigned, v); }
DI float bflo(unsigned u) { return __builtin_bit_cast(float, u << 16); }
DI float bfhi(unsigned u) { return __builtin_bit_cast(float, u & 0xffff0000u); }
DI float rbf(float a) { return (float)(__bf16)a; }
DI f32x16 mfma32(u32x4 a, u32x4 b, f32x16 c) {
  return __builtin_amdgcn_mfma_f32_32x32x16_bf16(__builtin_bit_cast(bf16x8, a), __builtin_bit_cast(bf16x8, b), c, 0, 0, 0);
}
DI f32x4 mfma16(u32x4 a, u32x4 b, f32x4 c) {
  return __builtin_amdgcn_mfma_f32_16x16x32_bf16(__builtin_bit_cast(bf16x8, a), __builtin_bit_cast(bf16x8, b), c, 0, 0, 0);
}
DI float cosr(float rev) { return __builtin_amdgcn_cosf(rev); }
DI float sinr(float rev) { return __builtin_amdgcn_sinf(rev); }
#define WAIT_V(n) asm volatile("s_waitcnt vmcnt(" #n ")" ::: "memory")
#define WAIT_L(n) asm volatile("s_waitcnt lgkmcnt(" #n ")" ::: "memory")
#define BAR() do { asm volatile("" ::: "memory"); __builtin_amdgcn_s_barrier(); asm volatile("" ::: "memory"); } while (0)
DI void dma16(rsrc_t r, unsigned ldsaddr, unsigned voff, unsigned soff) {
  asm volatile("s_mov_b32 m0, %0\n\ts_nop 0\n\tbuffer_load_dwordx4 %1, %2, %3 offen lds" :: "s"(ldsaddr), "v"(voff), "s"(r), "s"(soff) : "memory");
}
template <int AUX> DI void st128(u32x4 v, rsrc_t r, unsigned voff, unsigned uoff) { __builtin_amdgcn_raw_buffer_store_b128(v, r, voff + uoff, 0, AUX); }
DI u32x4 lds128(unsigned a) { return *(LDSP u32x4*)a; }
DI u32x2 ldstr(unsigned a) { return __builtin_bit_cast(u32x2, __builtin_amdgcn_ds_read_tr16_b64_v4i16((LDSP s16x4*)a)); }

constexpr unsigned STG = 49152, ST_OFF = 3 * STG, CS_OFF = ST_OFF + 1024, K1_LDS = CS_OFF + 1024;

struct Frag { u32x4 x[4]; u32x4 w[2]; };

__global__ __launch_bounds__(512, 2) void k1(const void* __restrict__ px, const void* __restrict__ pwg, const void* __restrict__ pwu,
                                             const void* __restrict__ pwr, const void* __restrict__ pwi, char* __restrict__ ws) {
  extern __shared__ __attribute__((aligned(16))) char lds_[];
  const unsigned lds = (unsigned)(size_t)(LDSP char*)lds_;
  const int bid = blockIdx.x, xcd = bid & 7, idx = bid >> 3;
  const int rb = (xcd & 1) * 8 + (idx & 7), cb = (xcd >> 1) * 4 + (idx >> 3);
  const bool dft = cb >= 14;
  const int wave = __builtin_amdgcn_readfirstlane(threadIdx.x >> 6);
  const unsigned lane = threadIdx.x & 63;

  if (wave < 4) {
    const int w = wave;
    const unsigned r = lane & 31, hh = lane >> 5;
    unsigned xa[4];
#pragma unroll
    for (int ks = 0; ks < 4; ks++) xa[ks] = lds + r * 128 + (((2 * ks + hh) ^ ((r >> 1) & 7)) << 4);
    const unsigned blk = (lane >> 4) & 1, q = (lane & 15) >> 2, p = lane & 3;
    const unsigned wa = lds + 16384 + (8 * hh + q) * 256 + (((4 * w + 2 * blk + (p >> 1)) ^ (q << 2)) << 4) + 8 * (p & 1);
    f32x16 acc[4][2];
#pragma unroll
    for (int t = 0; t < 4; t++)
#pragma unroll
      for (int m = 0; m < 2; m++)
#pragma unroll
        for (int i = 0; i < 16; i++) acc[t][m][i] = 0.f;
    auto load = [&](Frag& F, unsigned sb, int ks) {
#pragma unroll
      for (int m = 0; m < 2; m++) {
        u32x2 lo = ldstr(wa + sb + ks * 4096 + m * 16384), hi = ldstr(wa + sb + ks * 4096 + m * 16384 + 1024);
        F.w[m] = u32x4{lo[0], lo[1], hi[0], hi[1]};
      }
#pragma unroll
      for (int t = 0; t < 4; t++) F.x[t] = lds128(xa[ks] + sb + t * 4096);
    };
    auto mma = [&](const Frag& F) {
#pragma unroll
      for (int t = 0; t < 4; t++)
#pragma unroll
        for (int m = 0; m < 2; m++) acc[t][m] = mfma32(F.w[m], F.x[t], acc[t][m]);
    };
    auto sched = [&]() {
#pragma unroll
      for (int i = 0; i < 8; i++) {
        __builtin_amdgcn_sched_group_barrier(0x008, 1, 0);
        __builtin_amdgcn_sched_group_barrier(0x100, 1, 0);
      }
    };
    Frag A, B;
    unsigned sb = 0;
    BAR();
    load(A, sb, 0);
#pragma unroll 1
    for (int s = 0; s < 8; s++) {
      load(B, sb, 1); mma(A); sched(); __builtin_amdgcn_sched_barrier(0);
      load(A, sb, 2); mma(B); sched(); __builtin_amdgcn_sched_barrier(0);
      load(B, sb, 3); mma(A); sched(); __builtin_amdgcn_sched_barrier(0);
      WAIT_L(0);
      BAR();
      sb = (sb == 2 * STG) ? 0 : sb + STG;
      load(A, sb, 0);
      mma(B); sched(); __builtin_amdgcn_sched_barrier(0);
    }
    f32x4 cs[2][4];
#pragma unroll
    for (int m = 0; m < 2; m++)
#pragma unroll
      for (int g = 0; g < 4; g++) cs[m][g] = *(LDSP f32x4*)(lds + CS_OFF + (m * 128 + 32 * w + 8 * g + 4 * hh) * 4);
    f32x2 st[4];
#pragma unroll
    for (int t = 0; t < 4; t++) st[t] = *(LDSP f32x2*)(lds + ST_OFF + (32 * t + r) * 8);
    if (!dft) {
      const rsrc_t rH = mk_rsrc(ws + OFF_H, H_BYTES);
#pragma unroll
      for (int t = 0; t < 4; t++) {
        const float sd = st[t][0], mu = st[t][1], rr = __builtin_amdgcn_rcpf(sd);
        const float al = -2.302208198f * rr, be = -2.302208198f * 0.044715f * rr * rr * rr, v2 = sd * sd;
        float h[16];
#pragma unroll
        for (int i = 0; i < 16; i++) {
          const float g = acc[t][0][i] - mu * cs[0][i >> 2][i & 3];
          const float u = acc[t][1][i] - mu * cs[1][i >> 2][i & 3];
          const float e = __builtin_amdgcn_exp2f(g * (al + be * g * g));
          h[i] = g * u * __builtin_amdgcn_rcpf(v2 + v2 * e);
        }
#pragma unroll
        for (int s2 = 0; s2 < 2; s2++) {
          u32x4 o = {pk2(h[8 * s2], h[8 * s2 + 1]), pk2(h[8 * s2 + 2], h[8 * s2 + 3]), pk2(h[8 * s2 + 4], h[8 * s2 + 5]), pk2(h[8 * s2 + 6], h[8 * s2 + 7])};
          st128<AUX_WT>(o, rH, lane * 16, ((4 * rb + t) * 112 + cb * 8 + 2 * w + s2) * 1024);
        }
      }
    } else {
      const rsrc_t rT = mk_rsrc(ws + OFF_T, T_BYTES);
      float rr[4];
#pragma unroll
      for (int t = 0; t < 4; t++) rr[t] = __builtin_amdgcn_rcpf(st[t][0]);
#pragma unroll
      for (int i = 0; i < 16; i++) {
        u32x4 o;
#pragma unroll
        for (int t = 0; t < 4; t++) o[t] = pk2(rr[t] * (acc[t][0][i] - st[t][1] * cs[0][i >> 2][i & 3]), rr[t] * acc[t][1][i]);
        st128<AUX_WT>(o, rT, hh * 4 * 8192 + r * 16, ((cb - 14) * 128 + 32 * w + (i & 3) + 8 * (i >> 2)) * 8192 + rb * 512);
      }
    }
  } else {
    const int hw = wave - 4;
    const rsrc_t rX = mk_rsrc(px, X_BYTES), rG = mk_rsrc(pwg, W1_BYTES), rU = mk_rsrc(pwu, W1_BYTES);
    const unsigned xv0 = (lane >> 3) * 1024 + (((lane & 7) ^ (lane >> 4)) << 4), xv1 = xv0 ^ 64;
    const unsigned wv = (lane >> 4) * 3584 + (((lane & 15) ^ ((lane >> 4) << 2)) << 4);
    auto stage_x = [&](int s, unsigned sb) {
#pragma unroll
      for (int i = 0; i < 4; i++) dma16(rX, lds + sb + (4 * hw + i) * 1024, (i & 1) ? xv1 : xv0, (rb * 128 + (4 * hw + i) * 8) * 1024 + s * 128);
    };
    auto stage_w = [&](int s, unsigned sb) {
#pragma unroll
      for (int i = 0; i < 4; i++) {
        const unsigned so = (64 * s + 4 * (4 * hw + i)) * 3584 + cb * 256;
        dma16(rG, lds + sb + 16384 + (4 * hw + i) * 1024, wv, so);
        dma16(rU, lds + sb + 32768 + (4 * hw + i) * 1024, wv, so);
      }
    };
    f32x2 sx[4], sq[4];
#pragma unroll
    for (int i = 0; i < 4; i++) { sx[i] = f32x2{0.f, 0.f}; sq[i] = f32x2{0.f, 0.f}; }
    auto xstats = [&](unsigned sb) {
#pragma unroll
      for (int i = 0; i < 4; i++) {
        const u32x4 v = lds128(lds + sb + (4 * hw + i) * 1024 + lane * 16);
#pragma unroll
        for (int e = 0; e < 4; e++) { const f32x2 f = {bflo(v[e]), bfhi(v[e])}; sx[i] += f; sq[i] += f * f; }
      }
    };
    const unsigned f = threadIdx.x - 256;
    const rsrc_t rWr = mk_rsrc(pwr, WF_BYTES), rWi = mk_rsrc(pwi, WF_BYTES);
    const int k0 = 16 * (bid >> 6) + (bid & 15) + 256 * ((bid >> 4) & 3);
    unsigned short a_r[8], a_i[8], n_r = 0, n_i = 0;
    auto wt_load = [&]() {
#pragma unroll
      for (int j = 0; j < 8; j++) {
        const unsigned so = (k0 + 64 * (j & 3) + 1024 * (j >> 2)) * 514;
        a_r[j] = __builtin_amdgcn_raw_buffer_load_b16(rWr, f * 2, so, 0);
        a_i[j] = __builtin_amdgcn_raw_buffer_load_b16(rWi, f * 2, so, 0);
      }
      const unsigned vo = (k0 + 64 * (lane & 3) + 1024 * ((lane >> 2) & 1)) * 514 + 512;
      n_r = __builtin_amdgcn_raw_buffer_load_b16(rWr, vo, 0, 0);
      n_i = __builtin_amdgcn_raw_buffer_load_b16(rWi, vo, 0, 0);
    };
    if (!dft) {
      const unsigned qd = (lane >> 2) & 7;
      const unsigned crow = ((qd >> 2) << 1) | ((qd ^ (qd >> 1) ^ (qd >> 2)) & 1);
      const unsigned cch = (hw & 1) * 8 + (qd & 2) * 2 + (lane & 3);
      const unsigned ca = lds + 16384 + (hw >> 1) * 16384 + (crow + 4 * (lane >> 5)) * 256 + ((cch ^ (crow << 2)) << 4);
      f32x2 cacc[4];
#pragma unroll
      for (int e = 0; e < 4; e++) cacc[e] = f32x2{0.f, 0.f};
      auto cstats = [&](unsigned sb) {
#pragma unroll
        for (int i = 0; i < 8; i++) {
          const u32x4 v = lds128(ca + sb + i * 2048);
#pragma unroll
          for (int e = 0; e < 4; e++) cacc[e] += f32x2{bflo(v[e]), bfhi(v[e])};
        }
      };
      stage_x(0, 0); stage_w(0, 0); stage_x(1, STG); stage_w(1, STG); stage_x(2, 2 * STG); stage_w(2, 2 * STG);
      WAIT_V(24); BAR();
      xstats(0); cstats(0);
      WAIT_V(12); WAIT_L(0); BAR();
      unsigned sb = STG, fb = 0;
#pragma unroll 1
      for (int s = 1; s < 5; s++) {
        stage_x(s + 2, fb); stage_w(s + 2, fb);
        xstats(sb); cstats(sb);
        WAIT_V(12); WAIT_L(0); BAR();
        fb = sb; sb = (sb == 2 * STG) ? 0 : sb + STG;
      }
      stage_x(7, STG); stage_w(7, STG);
      wt_load();
      xstats(2 * STG); cstats(2 * STG);
      WAIT_V(30); WAIT_L(0); BAR();
      xstats(0); cstats(0);
      WAIT_V(18); WAIT_L(0); BAR();
      xstats(STG); cstats(STG);
#pragma unroll
      for (int e = 0; e < 4; e++)
#pragma unroll
        for (int z = 0; z < 2; z++) {
          float v = cacc[e][z];
          v += __shfl_xor(v, 4); v += __shfl_xor(v, 16); v += __shfl_xor(v, 32);
          cacc[e][z] = v;
        }
      if ((lane & 52) == 0) {
        const unsigned a = lds + CS_OFF + ((hw >> 1) * 128 + cch * 8) * 4;
        *(LDSP f32x4*)a = f32x4{cacc[0][0], cacc[0][1], cacc[1][0], cacc[1][1]};
        *(LDSP f32x4*)(a + 16) = f32x4{cacc[2][0], cacc[2][1], cacc[3][0], cacc[3][1]};
      }
    } else {
      const unsigned ch = (lane & 15) ^ ((lane >> 4) << 2);
      const unsigned f0 = (cb - 14) * 128 + ch * 8, kk = 4 * hw + (lane >> 4);
      float c[8], sn[8], rc[8], rs[8];
#pragma unroll
      for (int e = 0; e < 8; e++) {
        const float a0 = (float)((f0 + e) * kk) * (1.f / 512.f), a1 = (float)(f0 + e) * (1.f / 32.f);
        c[e] = cosr(a0); sn[e] = -sinr(a0); rc[e] = cosr(a1); rs[e] = -sinr(a1);
      }
      if (f0 == 0) sn[0] = (kk & 1) ? -1.f : 1.f;
      const unsigned fa = lds + 16384 + hw * 1024 + lane * 16;
      auto gen = [&](unsigned sb) {
#pragma unroll
        for (int i = 0; i < 4; i++) {
          *(LDSP u32x4*)(fa + sb + i * 4096) = u32x4{pk2(c[0], c[1]), pk2(c[2], c[3]), pk2(c[4], c[5]), pk2(c[6], c[7])};
          *(LDSP u32x4*)(fa + sb + i * 4096 + 16384) = u32x4{pk2(sn[0], sn[1]), pk2(sn[2], sn[3]), pk2(sn[4], sn[5]), pk2(sn[6], sn[7])};
#pragma unroll
          for (int e = 0; e < 8; e++) { const float t = c[e] * rc[e] - sn[e] * rs[e]; sn[e] = sn[e] * rc[e] + c[e] * rs[e]; c[e] = t; }
        }
      };
      stage_x(0, 0); stage_x(1, STG); stage_x(2, 2 * STG);
      gen(0);
      *(LDSP float*)(lds + CS_OFF + f * 4) = (f == 0 && cb == 14) ? 512.f : 0.f;
      WAIT_V(8); WAIT_L(0); BAR();
      gen(STG); xstats(0);
      WAIT_V(4); WAIT_L(0); BAR();
      unsigned sb = STG, fb = 0;
#pragma unroll 1
      for (int s = 1; s < 5; s++) {
        const unsigned nb = (sb == 2 * STG) ? 0 : sb + STG;
        stage_x(s + 2, fb);
        gen(nb); xstats(sb);
        WAIT_V(4); WAIT_L(0); BAR();
        fb = sb; sb = nb;
      }
      stage_x(7, STG);
      wt_load();
      gen(0); xstats(2 * STG);
      WAIT_V(22); WAIT_L(0); BAR();
      gen(STG); xstats(0);
      WAIT_V(18); WAIT_L(0); BAR();
      xstats(STG);
    }
#pragma unroll
    for (int i = 0; i < 4; i++) {
      float a = sx[i][0] + sx[i][1], b = sq[i][0] + sq[i][1];
      a += __shfl_xor(a, 1); b += __shfl_xor(b, 1);
      a += __shfl_xor(a, 2); b += __shfl_xor(b, 2);
      a += __shfl_xor(a, 4); b += __shfl_xor(b, 4);
      const float mu = a * (1.f / 512.f);
      const float var = fmaxf(b * (1.f / 512.f) - mu * mu, 0.f);
      const float sd = rbf(__builtin_sqrtf(rbf(rbf(var) + 1e-5f)));
      if ((lane & 7) == 0) *(LDSP f32x2*)(lds + ST_OFF + (32 * hw + 8 * i + (lane >> 3)) * 8) = f32x2{sd, rbf(mu)};
    }
    WAIT_L(0); BAR();
    const rsrc_t rWT = mk_rsrc(ws + OFF_WT, WT_BYTES);
    u32x4 o0, o1;
#pragma unroll
    for (int j = 0; j < 4; j++) { o0[j] = a_r[j] | ((unsigned)a_i[j] << 16); o1[j] = a_r[j + 4] | ((unsigned)a_i[j + 4] << 16); }
    st128<0>(o0, rWT, f * 8192, bid * 32);
    st128<0>(o1, rWT, f * 8192 + 16, bid * 32);
    if (f < 8) __builtin_amdgcn_raw_buffer_store_b32(n_r | ((unsigned)n_i << 16), rWT, 256 * 8192 + f * 4, bid * 32, 0);
  }
}

constexpr unsigned UROW = 272;
constexpr unsigned KF_LDS = 16384;

DI void col_fft(char* ws, int f, int w, unsigned lane, unsigned lds) {
  const rsrc_t rT = mk_rsrc(ws + OFF_T, T_BYTES), rWT = mk_rsrc(ws + OFF_WT, WT_BYTES), rZ = mk_rsrc(ws + OFF_Z, T_BYTES);
  const unsigned c = lane & 15, q = lane >> 4;
  u32x4 xa[2][4];
#pragma unroll
  for (int j = 0; j < 4; j++)
#pragma unroll
    for (int ta = 0; ta < 2; ta++) xa[ta][j] = __builtin_amdgcn_raw_buffer_load_b128(rT, q * 512 + c * 16 + j * 2048 + ta * 256, f * 8192, 0);
  u32x4 wv[2];
#pragma unroll
  for (int t = 0; t < 2; t++) wv[t] = __builtin_amdgcn_raw_buffer_load_b128(rWT, lane * 32 + t * 16, f * 8192 + w * 2048, 0);
  const unsigned k1 = 16 * w + c;
  u32x4 bre[4], bim[4];
#pragma unroll
  for (int j = 0; j < 4; j++)
#pragma unroll
    for (int e = 0; e < 4; e++) {
      const float a = (float)(k1 * (16 * j + 4 * q + e)) * (1.f / 64.f);
      const float co = cosr(a), si = sinr(a);
      bre[j][e] = pk2(co, si); bim[j][e] = pk2(-si, co);
    }
  u32x4 c32[2], s32[2], n32[2];
  {
    float co[4], si[4];
#pragma unroll
    for (int e = 0; e < 4; e++) { const float a = (float)(c * (4 * q + e)) * (1.f / 32.f); co[e] = cosr(a); si[e] = sinr(a); }
    const unsigned sg = (c & 1) ? 0x80008000u : 0u;
    const unsigned c01 = pk2(co[0], co[1]), c23 = pk2(co[2], co[3]), s01 = pk2(si[0], si[1]), s23 = pk2(si[2], si[3]);
    c32[0] = u32x4{c01, c23, c01 ^ sg, c23 ^ sg};
    s32[0] = u32x4{s01, s23, s01 ^ sg, s23 ^ sg};
    c32[1] = c32[0] ^ 0x80000000u; s32[1] = s32[0] ^ 0x80000000u;
    n32[0] = s32[0] ^ 0x80008000u; n32[1] = s32[1] ^ 0x80008000u;
  }
  float twc[2][4], tws[2][4];
#pragma unroll
  for (int ta = 0; ta < 2; ta++)
#pragma unroll
    for (int e = 0; e < 4; e++) { const float a = (float)(k1 * (16 * ta + 4 * q + e)) * (1.f / 2048.f); twc[ta][e] = cosr(a); tws[ta][e] = sinr(a); }
  const f32x4 z4 = {0.f, 0.f, 0.f, 0.f};
  f32x4 yr[2] = {z4, z4}, yi[2] = {z4, z4};
#pragma unroll
  for (int j = 0; j < 4; j++)
#pragma unroll
    for (int ta = 0; ta < 2; ta++) { yr[ta] = mfma16(xa[ta][j], bre[j], yr[ta]); yi[ta] = mfma16(xa[ta][j], bim[j], yi[ta]); }
  u32x4 pr, pi;
#pragma unroll
  for (int ta = 0; ta < 2; ta++) {
    float a[4], b[4];
#pragma unroll
    for (int e = 0; e < 4; e++) { a[e] = yr[ta][e] * twc[ta][e] + yi[ta][e] * tws[ta][e]; b[e] = yi[ta][e] * twc[ta][e] - yr[ta][e] * tws[ta][e]; }
    pr[2 * ta] = pk2(a[0], a[1]); pr[2 * ta + 1] = pk2(a[2], a[3]); pi[2 * ta] = pk2(b[0], b[1]); pi[2 * ta + 1] = pk2(b[2], b[3]);
  }
  f32x4 xr[2], xi[2];
#pragma unroll
  for (int t = 0; t < 2; t++) {
    xr[t] = mfma16(c32[t], pr, z4); xr[t] = mfma16(s32[t], pi, xr[t]);
    xi[t] = mfma16(c32[t], pi, z4); xi[t] = mfma16(n32[t], pr, xi[t]);
  }
  float fr[2][4], fi[2][4];
  if (f != 0) {
    const float sc = 1.f / 524288.f;
#pragma unroll
    for (int t = 0; t < 2; t++)
#pragma unroll
      for (int e = 0; e < 4; e++) {
        const float a = bflo(wv[t][e]) * sc, b = bfhi(wv[t][e]) * sc;
        fr[t][e] = xr[t][e] * a - xi[t][e] * b; fi[t][e] = xr[t][e] * b + xi[t][e] * a;
      }
  } else {
#pragma unroll
    for (int t = 0; t < 2; t++)
#pragma unroll
      for (int e = 0; e < 4; e++) *(LDSP f32x2*)(lds + (k1 + 64 * (16 * t + 4 * q + e)) * 8) = f32x2{xr[t][e], xi[t][e]};
    WAIT_L(0); BAR();
    const float sc = 0.25f / 1048576.f;
#pragma unroll
    for (int t = 0; t < 2; t++)
#pragma unroll
      for (int e = 0; e < 4; e++) {
        const unsigned k = k1 + 64 * (16 * t + 4 * q + e), kn = (2048 - k) & 2047;
        const unsigned m1 = kn & 63, m2 = kn >> 6;
        const unsigned pos = ((m1 >> 4) * 64 + ((m2 >> 2) & 3) * 16 + (m1 & 15)) * 32 + ((m2 >> 4) * 4 + (m2 & 3)) * 4;
        const unsigned d0 = wv[t][e], d0n = __builtin_amdgcn_raw_buffer_load_b32(rWT, pos, 0, 0);
        const unsigned d1 = __builtin_amdgcn_raw_buffer_load_b32(rWT, lane * 32 + t * 16 + e * 4, 256 * 8192 + w * 2048, 0);
        const unsigned d1n = __builtin_amdgcn_raw_buffer_load_b32(rWT, pos, 256 * 8192, 0);
        const f32x2 pn = *(LDSP f32x2*)(lds + kn * 8);
        const float ar = bflo(d0) + bflo(d0n), ai = bfhi(d0) - bfhi(d0n), br = bflo(d1) + bflo(d1n), bi = bfhi(d1) - bfhi(d1n);
        const float war = (ar + br) * sc, wai = (ai + bi) * sc, wbr = (ar - br) * sc, wbi = (ai - bi) * sc;
        fr[t][e] = xr[t][e] * war - xi[t][e] * wai + pn[0] * wbr + pn[1] * wbi;
        fi[t][e] = xr[t][e] * wai + xi[t][e] * war + pn[0] * wbi - pn[1] * wbr;
      }
    WAIT_L(0); BAR();
  }
#pragma unroll
  for (int t = 0; t < 2; t++) {
    pr[2 * t] = pk2(fr[t][0], fr[t][1]); pr[2 * t + 1] = pk2(fr[t][2], fr[t][3]);
    pi[2 * t] = pk2(fi[t][0], fi[t][1]); pi[2 * t + 1] = pk2(fi[t][2], fi[t][3]);
  }
#pragma unroll
  for (int ta = 0; ta < 2; ta++) {
    f32x4 ur = mfma16(c32[ta], pr, z4); ur = mfma16(n32[ta], pi, ur);
    f32x4 ui = mfma16(c32[ta], pi, z4); ui = mfma16(s32[ta], pr, ui);
#pragma unroll
    for (int e = 0; e < 4; e++) {
      const float a = ur[e] * twc[ta][e] - ui[e] * tws[ta][e], b = ui[e] * twc[ta][e] + ur[e] * tws[ta][e];
      *(LDSP unsigned*)(lds + (16 * ta + 4 * q + e) * UROW + k1 * 4) = pk2(a, -b);
    }
  }
  WAIT_L(0); BAR();
#pragma unroll
  for (int ta = 0; ta < 2; ta++) {
    f32x4 zr = z4, zi = z4;
#pragma unroll
    for (int j = 0; j < 4; j++) {
      const u32x4 u = lds128(lds + (16 * ta + c) * UROW + (16 * j + 4 * q) * 4);
      zr = mfma16(bre[j], u, zr); zi = mfma16(bim[j], u, zi);
    }
    const u32x4 o = {pk2(zr[0], zi[0]), pk2(zr[1], zi[1]), pk2(zr[2], zi[2]), pk2(zr[3], zi[3])};
    st128<AUX_WT>(o, rZ, q * 512 + c * 16 + ta * 256, f * 8192 + w * 2048);
  }
}

__global__ __launch_bounds__(256) void kf(char* __restrict__ ws) {
  __shared__ __attribute__((aligned(16))) char lds_[KF_LDS];
  col_fft(ws, blockIdx.x, __builtin_amdgcn_readfirstlane(threadIdx.x >> 6), threadIdx.x & 63, (unsigned)(size_t)(LDSP char*)lds_);
}

constexpr unsigned K2_LDS = 131072;
constexpr int PF = 4;

__global__ __launch_bounds__(512, 2) void k2(const void* __restrict__ px, const void* __restrict__ pwd, char* __restrict__ ws, void* __restrict__ pout) {
  extern __shared__ __attribute__((aligned(16))) char lds_[];
  const unsigned lds = (unsigned)(size_t)(LDSP char*)lds_;
  const int bid = blockIdx.x, xcd = bid & 7, idx = bid >> 3;
  const int mt = xcd * 4 + (idx & 3), nb = idx >> 2;
  const int rb = mt >> 1, half = mt & 1;
  const int wave = __builtin_amdgcn_readfirstlane(threadIdx.x >> 6);
  const unsigned lane = threadIdx.x & 63, hh = lane >> 5, rp = (lane >> 4) & 1, c = lane & 15, l32 = lane & 31;
  f32x16 acc[2][2];
#pragma unroll
  for (int u = 0; u < 2; u++)
#pragma unroll
    for (int v = 0; v < 2; v++)
#pragma unroll
      for (int i = 0; i < 16; i++) acc[u][v][i] = 0.f;
  if (wave < 4) {
    const rsrc_t rH = mk_rsrc(ws + OFF_H, H_BYTES), rW = mk_rsrc(pwd, W1_BYTES);
    const unsigned av = rp * 2 * 112 * 1024 + (hh * 32 + 16 * half + c) * 16;
    const unsigned bv = hh * 4096 + l32 * 4;
    u32x4 a[PF][2]; unsigned b[PF][8];
    auto ld = [&](int slot, int ks) {
#pragma unroll
      for (int u = 0; u < 2; u++) a[slot][u] = __builtin_amdgcn_raw_buffer_load_b128(rH, av, ((4 * rb + u) * 112 + ks) * 1024, 0);
#pragma unroll
      for (int j = 0; j < 8; j++) b[slot][j] = __builtin_amdgcn_raw_buffer_load_b32(rW, bv + (j & 3) * 1024, (16 * ks + 8 * (j >> 2)) * 1024 + nb * 128, 0);
    };
    auto mm = [&](int slot) {
      u32x4 be, bo;
#pragma unroll
      for (int j = 0; j < 4; j++) {
        be[j] = __builtin_amdgcn_perm(b[slot][2 * j + 1], b[slot][2 * j], 0x05040100u);
        bo[j] = __builtin_amdgcn_perm(b[slot][2 * j + 1], b[slot][2 * j], 0x07060302u);
      }
#pragma unroll
      for (int u = 0; u < 2; u++) { acc[u][0] = mfma32(a[slot][u], be, acc[u][0]); acc[u][1] = mfma32(a[slot][u], bo, acc[u][1]); }
    };
    const int ks0 = 28 * wave;
#pragma unroll
    for (int i = 0; i < PF; i++) ld(i, ks0 + i);
#pragma unroll 1
    for (int k = PF; k < 28; k += PF) {
#pragma unroll
      for (int i = 0; i < PF; i++) { mm(i); __builtin_amdgcn_sched_barrier(0); ld(i, ks0 + k + i); __builtin_amdgcn_sched_barrier(0); }
    }
#pragma unroll
    for (int i = 0; i < PF; i++) mm(i);
  } else {
    const int wz = wave - 4;
    const rsrc_t rZ = mk_rsrc(ws + OFF_Z, T_BYTES);
    u32x2 z[8][4];
    const unsigned zv = hh * 4 * 8192 + (16 * half + c) * 16 + 8 * rp;
#pragma unroll
    for (int ks = 0; ks < 8; ks++)
#pragma unroll
      for (int j = 0; j < 4; j++) z[ks][j] = __builtin_amdgcn_raw_buffer_load_b64(rZ, zv, (64 * wz + 8 * ks + j) * 8192 + rb * 512, 0);
    const float d0 = (float)(64 * nb + 2 * l32) * (1.f / 512.f), d1 = d0 + (1.f / 512.f);
    const float fb = (float)(64 * wz + 4 * hh);
#pragma unroll
    for (int ks = 0; ks < 8; ks++) {
      u32x4 be, bo;
#pragma unroll
      for (int j = 0; j < 4; j++) {
        const float ff = fb + (float)(8 * ks + j);
        be[j] = pk2(cosr(ff * d0), sinr(ff * d0)); bo[j] = pk2(cosr(ff * d1), sinr(ff * d1));
      }
      if (ks == 0 && wz == 0 && hh == 0) { be[0] = 0xbf803f80u; bo[0] = 0x3f803f80u; }
#pragma unroll
      for (int u = 0; u < 2; u++) {
        const u32x4 a = {z[ks][0][u], z[ks][1][u], z[ks][2][u], z[ks][3][u]};
        acc[u][0] = mfma32(a, be, acc[u][0]); acc[u][1] = mfma32(a, bo, acc[u][1]);
      }
    }
  }
  const int u0 = wave >> 2, g0 = wave & 3;
  const rsrc_t rX = mk_rsrc(px, X_BYTES), rO = mk_rsrc(pout, X_BYTES);
  const unsigned so = (128 * rb + 32 * (2 * (g0 >> 1) + u0) + 16 * half + 8 * (g0 & 1)) * 1024 + nb * 128;
  const unsigned ov = hh * 4096 + l32 * 4;
  unsigned xr[4];
#pragma unroll
  for (int e = 0; e < 4; e++) xr[e] = __builtin_amdgcn_raw_buffer_load_b32(rX, ov + e * 1024, so, 0);
#pragma unroll
  for (int u = 0; u < 2; u++)
#pragma unroll
    for (int v = 0; v < 2; v++)
#pragma unroll
      for (int g = 0; g < 4; g++)
        *(LDSP f32x4*)(lds + wave * 16384 + ((u * 2 + v) * 4 + g) * 1024 + lane * 16) = f32x4{acc[u][v][4 * g], acc[u][v][4 * g + 1], acc[u][v][4 * g + 2], acc[u][v][4 * g + 3]};
  WAIT_L(0); BAR();
  f32x4 s0 = {0.f, 0.f, 0.f, 0.f}, s1 = s0;
#pragma unroll
  for (int pw = 0; pw < 8; pw++) {
    s0 += *(LDSP f32x4*)(lds + pw * 16384 + ((u0 * 2 + 0) * 4 + g0) * 1024 + lane * 16);
    s1 += *(LDSP f32x4*)(lds + pw * 16384 + ((u0 * 2 + 1) * 4 + g0) * 1024 + lane * 16);
  }
#pragma unroll
  for (int e = 0; e < 4; e++) __builtin_amdgcn_raw_buffer_store_b32(pk2(bflo(xr[e]) + s0[e], bfhi(xr[e]) + s1[e]), rO, ov + e * 1024, so, 0);
}

extern "C" void kernel_launch(void* const* d_in, const int* in_sizes, int n_in, void* d_out, int out_size, void* d_ws, size_t ws_size,
                              hipStream_t stream) {
  (void)in_sizes; (void)n_in; (void)out_size; (void)ws_size;
  (void)hipFuncSetAttribute((const void*)k1, hipFuncAttributeMaxDynamicSharedMemorySize, K1_LDS);
  (void)hipFuncSetAttribute((const void*)k2, hipFuncAttributeMaxDynamicSharedMemorySize, K2_LDS);
  k1<<<256, 512, K1_LDS, stream>>>(d_in[0], d_in[4], d_in[5], d_in[1], d_in[2], (char*)d_ws);
  kf<<<256, 256, 0, stream>>>((char*)d_ws);
  k2<<<256, 512, K2_LDS, stream>>>(d_in[0], d_in[6], (char*)d_ws, d_out);
}
```

```cpp
#define DIAG 1
#include <hip/hip_runtime.h>
#include <hip/hip_bf16.h>
#include <cstdio>
#pragma clang diagnostic ignored "-Wint-to-pointer-cast"

typedef __bf16 bf16x8 __attribute__((ext_vector_type(8)));
typedef __bf16 bf16x2 __attribute__((ext_vector_type(2)));
typedef short s16x4 __attribute__((ext_vector_type(4)));
typedef short s16x8 __attribute__((ext_vector_type(8)));
typedef float f32x16 __attribute__((ext_vector_type(16)));
typedef float f32x4 __attribute__((ext_vector_type(4)));
typedef float f32x2 __attribute__((ext_vector_type(2)));
typedef unsigned u32x4 __attribute__((ext_vector_type(4)));
typedef unsigned u32x2 __attribute__((ext_vector_type(2)));
#define LDSP __attribute__((address_space(3)))
#define DI __device__ __forceinline__
typedef __amdgpu_buffer_rsrc_t rsrc_t;

constexpr unsigned X_BYTES = 2048u * 512u * 2u;
constexpr unsigned W1_BYTES = 512u * 1792u * 2u;
constexpr unsigned WF_BYTES = 2048u * 257u * 2u;
constexpr unsigned H_BYTES = 2048u * 1792u * 2u;
constexpr unsigned T_BYTES = 256u * 8192u;
constexpr unsigned WT_BYTES = 257u * 8192u;
constexpr unsigned OFF_H = 0, OFF_T = H_BYTES, OFF_Z = OFF_T + T_BYTES, OFF_WT = OFF_Z + T_BYTES;
constexpr int AUX_WT = 16;

DI rsrc_t mk_rsrc(const void* p, unsigned bytes) { return __builtin_amdgcn_make_buffer_rsrc((void*)p, 0, bytes, 0x00020000); }
DI unsigned pk2(float a, float b) { bf16x2 v = {(__bf16)a, (__bf16)b}; return __builtin_bit_cast(unsigned, v); }
DI float bflo(unsigned u) { return __builtin_bit_cast(float, u << 16); }
DI float bfhi(unsigned u) { return __builtin_bit_cast(float, u & 0xffff0000u); }
DI float rbf(float a) { return (float)(__bf16)a; }
DI f32x16 mfma32(u32x4 a, u32x4 b, f32x16 c) {
  return __builtin_amdgcn_mfma_f32_32x32x16_bf16(__builtin_bit_cast(bf16x8, a), __builtin_bit_cast(bf16x8, b), c, 0, 0, 0);
}
DI f32x4 mfma16(u32x4 a, u32x4 b, f32x4 c) {
  return __builtin_amdgcn_mfma_f32_16x16x32_bf16(__builtin_bit_cast(bf16x8, a), __builtin_bit_cast(bf16x8, b), c, 0, 0, 0);
}
DI float cosr(float rev) { return __builtin_amdgcn_cosf(rev); }
DI float sinr(float rev) { return __builtin_amdgcn_sinf(rev); }
#define WAIT_V(n) asm volatile("s_waitcnt vmcnt(" #n ")" ::: "memory")
#define WAIT_L(n) asm volatile("s_waitcnt lgkmcnt(" #n ")" ::: "memory")
#define BAR() do { asm volatile("" ::: "memory"); __builtin_amdgcn_s_barrier(); asm volatile("" ::: "memory"); } while (0)
DI void dma16(rsrc_t r, unsigned ldsaddr, unsigned voff, unsigned soff) {
  asm volatile("s_mov_b32 m0, %0\n\ts_nop 0\n\tbuffer_load_dwordx4 %1, %2, %3 offen lds" :: "s"(ldsaddr), "v"(voff), "s"(r), "s"(soff) : "memory");
}
template <int AUX> DI void st128(u32x4 v, rsrc_t r, unsigned voff, unsigned uoff) { __builtin_amdgcn_raw_buffer_store_b128(v, r, voff + uoff, 0, AUX); }
DI u32x4 lds128(unsigned a) { return *(LDSP u32x4*)a; }
DI u32x2 ldstr(unsigned a) { return __builtin_bit_cast(u32x2, __builtin_amdgcn_ds_read_tr16_b64_v4i16((LDSP s16x4*)a)); }

#define DIAG_OFF (64u << 20)
#define STAMP(ldsbase, i) do { unsigned t_ = (unsigned)__builtin_amdgcn_s_memrealtime(); *(LDSP volatile unsigned*)((ldsbase) + (threadIdx.x >> 6) * 128 + (i) * 4) = t_; } while (0)
#define FLUSH(ldsbase, kern) do { asm volatile("s_waitcnt vmcnt(0) lgkmcnt(0)" ::: "memory"); STAMP(ldsbase, 31); asm volatile("s_waitcnt lgkmcnt(0)" ::: "memory"); if ((threadIdx.x & 63) < 32) ((unsigned*)(ws + DIAG_OFF))[(((kern) * 256 + blockIdx.x) * 8 + (threadIdx.x >> 6)) * 32 + (threadIdx.x & 63)] = *(LDSP volatile unsigned*)((ldsbase) + (threadIdx.x >> 6) * 128 + (threadIdx.x & 63) * 4); } while (0)
constexpr unsigned STG = 49152, ST_OFF = 3 * STG, CS_OFF = ST_OFF + 1024, DG1 = CS_OFF + 1024, K1_LDS = DG1 + 1024;

struct Frag { u32x4 x[4]; u32x4 w[2]; };

__global__ __launch_bounds__(512, 2) void k1(const void* __restrict__ px, const void* __restrict__ pwg, const void* __restrict__ pwu,
                                             const void* __restrict__ pwr, const void* __restrict__ pwi, char* __restrict__ ws) {
  extern __shared__ __attribute__((aligned(16))) char lds_[];
  const unsigned lds = (unsigned)(size_t)(LDSP char*)lds_;
  const int bid = blockIdx.x, xcd = bid & 7, idx = bid >> 3;
  const int rb = (xcd & 1) * 8 + (idx & 7), cb = (xcd >> 1) * 4 + (idx >> 3);
  const bool dft = cb >= 14;
  const int wave = __builtin_amdgcn_readfirstlane(threadIdx.x >> 6);
  const unsigned lane = threadIdx.x & 63;

  STAMP(lds + DG1, 0);
  if (wave < 4) {
    const int w = wave;
    const unsigned r = lane & 31, hh = lane >> 5;
    unsigned xa[4];
#pragma unroll
    for (int ks = 0; ks < 4; ks++) xa[ks] = lds + r * 128 + (((2 * ks + hh) ^ ((r >> 1) & 7)) << 4);
    const unsigned blk = (lane >> 4) & 1, q = (lane & 15) >> 2, p = lane & 3;
    const unsigned wa = lds + 16384 + (8 * hh + q) * 256 + (((4 * w + 2 * blk + (p >> 1)) ^ (q << 2)) << 4) + 8 * (p & 1);
    f32x16 acc[4][2];
#pragma unroll
    for (int t = 0; t < 4; t++)
#pragma unroll
      for (int m = 0; m < 2; m++)
#pragma unroll
        for (int i = 0; i < 16; i++) acc[t][m][i] = 0.f;
    auto load = [&](Frag& F, unsigned sb, int ks) {
#pragma unroll
      for (int m = 0; m < 2; m++) {
        u32x2 lo = ldstr(wa + sb + ks * 4096 + m * 16384), hi = ldstr(wa + sb + ks * 4096 + m * 16384 + 1024);
        F.w[m] = u32x4{lo[0], lo[1], hi[0], hi[1]};
      }
#pragma unroll
      for (int t = 0; t < 4; t++) F.x[t] = lds128(xa[ks] + sb + t * 4096);
    };
    auto mma = [&](const Frag& F) {
#pragma unroll
      for (int t = 0; t < 4; t++)
#pragma unroll
        for (int m = 0; m < 2; m++) acc[t][m] = mfma32(F.w[m], F.x[t], acc[t][m]);
    };
    auto sched = [&]() {
#pragma unroll
      for (int i = 0; i < 8; i++) {
        __builtin_amdgcn_sched_group_barrier(0x008, 1, 0);
        __builtin_amdgcn_sched_group_barrier(0x100, 1, 0);
      }
    };
    Frag A, B;
    unsigned sb = 0;
    BAR();
    STAMP(lds + DG1, 1);
    load(A, sb, 0);
#pragma unroll 1
    for (int s = 0; s < 8; s++) {
      load(B, sb, 1); mma(A); sched(); __builtin_amdgcn_sched_barrier(0);
      load(A, sb, 2); mma(B); sched(); __builtin_amdgcn_sched_barrier(0);
      load(B, sb, 3); mma(A); sched(); __builtin_amdgcn_sched_barrier(0);
      WAIT_L(0);
      STAMP(lds + DG1, 2 + 2 * s);
      BAR();
      STAMP(lds + DG1, 3 + 2 * s);
      sb = (sb == 2 * STG) ? 0 : sb + STG;
      load(A, sb, 0);
      mma(B); sched(); __builtin_amdgcn_sched_barrier(0);
    }
    STAMP(lds + DG1, 18);
    f32x4 cs[2][4];
#pragma unroll
    for (int m = 0; m < 2; m++)
#pragma unroll
      for (int g = 0; g < 4; g++) cs[m][g] = *(LDSP f32x4*)(lds + CS_OFF + (m * 128 + 32 * w + 8 * g + 4 * hh) * 4);
    f32x2 st[4];
#pragma unroll
    for (int t = 0; t < 4; t++) st[t] = *(LDSP f32x2*)(lds + ST_OFF + (32 * t + r) * 8);
    if (!dft) {
      const rsrc_t rH = mk_rsrc(ws + OFF_H, H_BYTES);
#pragma unroll
      for (int t = 0; t < 4; t++) {
        const float sd = st[t][0], mu = st[t][1], rr = __builtin_amdgcn_rcpf(sd);
        const float al = -2.302208198f * rr, be = -2.302208198f * 0.044715f * rr * rr * rr, v2 = sd * sd;
        float h[16];
#pragma unroll
        for (int i = 0; i < 16; i++) {
          const float g = acc[t][0][i] - mu * cs[0][i >> 2][i & 3];
          const float u = acc[t][1][i] - mu * cs[1][i >> 2][i & 3];
          const float e = __builtin_amdgcn_exp2f(g * (al + be * g * g));
          h[i] = g * u * __builtin_amdgcn_rcpf(v2 + v2 * e);
        }
#pragma unroll
        for (int s2 = 0; s2 < 2; s2++) {
          u32x4 o = {pk2(h[8 * s2], h[8 * s2 + 1]), pk2(h[8 * s2 + 2], h[8 * s2 + 3]), pk2(h[8 * s2 + 4], h[8 * s2 + 5]), pk2(h[8 * s2 + 6], h[8 * s2 + 7])};
          st128<AUX_WT>(o, rH, lane * 16, ((4 * rb + t) * 112 + cb * 8 + 2 * w + s2) * 1024);
        }
      }
    } else {
      const rsrc_t rT = mk_rsrc(ws + OFF_T, T_BYTES);
      float rr[4];
#pragma unroll
      for (int t = 0; t < 4; t++) rr[t] = __builtin_amdgcn_rcpf(st[t][0]);
#pragma unroll
      for (int i = 0; i < 16; i++) {
        u32x4 o;
#pragma unroll
        for (int t = 0; t < 4; t++) o[t] = pk2(rr[t] * (acc[t][0][i] - st[t][1] * cs[0][i >> 2][i & 3]), rr[t] * acc[t][1][i]);
        st128<AUX_WT>(o, rT, hh * 4 * 8192 + r * 16, ((cb - 14) * 128 + 32 * w + (i & 3) + 8 * (i >> 2)) * 8192 + rb * 512);
      }
    }
    STAMP(lds + DG1, 19);
  } else {
    const int hw = wave - 4;
    const rsrc_t rX = mk_rsrc(px, X_BYTES), rG = mk_rsrc(pwg, W1_BYTES), rU = mk_rsrc(pwu, W1_BYTES);
    const unsigned xv0 = (lane >> 3) * 1024 + (((lane & 7) ^ (lane >> 4)) << 4), xv1 = xv0 ^ 64;
    const unsigned wv = (lane >> 4) * 3584 + (((lane & 15) ^ ((lane >> 4) << 2)) << 4);
    auto stage_x = [&](int s, unsigned sb) {
#pragma unroll
      for (int i = 0; i < 4; i++) dma16(rX, lds + sb + (4 * hw + i) * 1024, (i & 1) ? xv1 : xv0, (rb * 128 + (4 * hw + i) * 8) * 1024 + s * 128);
    };
    auto stage_w = [&](int s, unsigned sb) {
#pragma unroll
      for (int i = 0; i < 4; i++) {
        const unsigned so = (64 * s + 4 * (4 * hw + i)) * 3584 + cb * 256;
        dma16(rG, lds + sb + 16384 + (4 * hw + i) * 1024, wv, so);
        dma16(rU, lds + sb + 32768 + (4 * hw + i) * 1024, wv, so);
      }
    };
    f32x2 sx[4], sq[4];
#pragma unroll
    for (int i = 0; i < 4; i++) { sx[i] = f32x2{0.f, 0.f}; sq[i] = f32x2{0.f, 0.f}; }
    auto xstats = [&](unsigned sb) {
#pragma unroll
      for (int i = 0; i < 4; i++) {
        const u32x4 v = lds128(lds + sb + (4 * hw + i) * 1024 + lane * 16);
#pragma unroll
        for (int e = 0; e < 4; e++) { const f32x2 f = {bflo(v[e]), bfhi(v[e])}; sx[i] += f; sq[i] += f * f; }
      }
    };
    const unsigned f = threadIdx.x - 256;
    const rsrc_t rWr = mk_rsrc(pwr, WF_BYTES), rWi = mk_rsrc(pwi, WF_BYTES);
    const int k0 = 16 * (bid >> 6) + (bid & 15) + 256 * ((bid >> 4) & 3);
    unsigned short a_r[8], a_i[8], n_r = 0, n_i = 0;
    auto wt_load = [&]() {
#pragma unroll
      for (int j = 0; j < 8; j++) {
        const unsigned so = (k0 + 64 * (j & 3) + 1024 * (j >> 2)) * 514;
        a_r[j] = __builtin_amdgcn_raw_buffer_load_b16(rWr, f * 2, so, 0);
        a_i[j] = __builtin_amdgcn_raw_buffer_load_b16(rWi, f * 2, so, 0);
      }
      const unsigned vo = (k0 + 64 * (lane & 3) + 1024 * ((lane >> 2) & 1)) * 514 + 512;
      n_r = __builtin_amdgcn_raw_buffer_load_b16(rWr, vo, 0, 0);
      n_i = __builtin_amdgcn_raw_buffer_load_b16(rWi, vo, 0, 0);
    };
    if (!dft) {
      const unsigned qd = (lane >> 2) & 7;
      const unsigned crow = ((qd >> 2) << 1) | ((qd ^ (qd >> 1) ^ (qd >> 2)) & 1);
      const unsigned cch = (hw & 1) * 8 + (qd & 2) * 2 + (lane & 3);
      const unsigned ca = lds + 16384 + (hw >> 1) * 16384 + (crow + 4 * (lane >> 5)) * 256 + ((cch ^ (crow << 2)) << 4);
      f32x2 cacc[4];
#pragma unroll
      for (int e = 0; e < 4; e++) cacc[e] = f32x2{0.f, 0.f};
      auto cstats = [&](unsigned sb) {
#pragma unroll
        for (int i = 0; i < 8; i++) {
          const u32x4 v = lds128(ca + sb + i * 2048);
#pragma unroll
          for (int e = 0; e < 4; e++) cacc[e] += f32x2{bflo(v[e]), bfhi(v[e])};
        }
      };
      stage_x(0, 0); stage_w(0, 0); stage_x(1, STG); stage_w(1, STG); stage_x(2, 2 * STG); stage_w(2, 2 * STG);
      STAMP(lds + DG1, 20);
      WAIT_V(24); STAMP(lds + DG1, 1); BAR();
      xstats(0); cstats(0);
      STAMP(lds + DG1, 2); WAIT_V(12); WAIT_L(0); STAMP(lds + DG1, 3); BAR();
      unsigned sb = STG, fb = 0;
#pragma unroll 1
      for (int s = 1; s < 5; s++) {
        stage_x(s + 2, fb); stage_w(s + 2, fb);
        STAMP(lds + DG1, 20 + s); xstats(sb); cstats(sb);
        STAMP(lds + DG1, 2 + 2 * s); WAIT_V(12); WAIT_L(0); STAMP(lds + DG1, 3 + 2 * s); BAR();
        fb = sb; sb = (sb == 2 * STG) ? 0 : sb + STG;
      }
      stage_x(7, STG); stage_w(7, STG);
      wt_load();
      STAMP(lds + DG1, 25); xstats(2 * STG); cstats(2 * STG);
      STAMP(lds + DG1, 12); WAIT_V(30); WAIT_L(0); STAMP(lds + DG1, 13); BAR();
      xstats(0); cstats(0);
      STAMP(lds + DG1, 14); WAIT_V(18); WAIT_L(0); STAMP(lds + DG1, 15); BAR();
      xstats(STG); cstats(STG); STAMP(lds + DG1, 16);
#pragma unroll
      for (int e = 0; e < 4; e++)
#pragma unroll
        for (int z = 0; z < 2; z++) {
          float v = cacc[e][z];
          v += __shfl_xor(v, 4); v += __shfl_xor(v, 16); v += __shfl_xor(v, 32);
          cacc[e][z] = v;
        }
      if ((lane & 52) == 0) {
        const unsigned a = lds + CS_OFF + ((hw >> 1) * 128 + cch * 8) * 4;
        *(LDSP f32x4*)a = f32x4{cacc[0][0], cacc[0][1], cacc[1][0], cacc[1][1]};
        *(LDSP f32x4*)(a + 16) = f32x4{cacc[2][0], cacc[2][1], cacc[3][0], cacc[3][1]};
      }
    } else {
      const unsigned ch = (lane & 15) ^ ((lane >> 4) << 2);
      const unsigned f0 = (cb - 14) * 128 + ch * 8, kk = 4 * hw + (lane >> 4);
      float c[8], sn[8], rc[8], rs[8];
#pragma unroll
      for (int e = 0; e < 8; e++) {
        const float a0 = (float)((f0 + e) * kk) * (1.f / 512.f), a1 = (float)(f0 + e) * (1.f / 32.f);
        c[e] = cosr(a0); sn[e] = -sinr(a0); rc[e] = cosr(a1); rs[e] = -sinr(a1);
      }
      if (f0 == 0) sn[0] = (kk & 1) ? -1.f : 1.f;
      const unsigned fa = lds + 16384 + hw * 1024 + lane * 16;
      auto gen = [&](unsigned sb) {
#pragma unroll
        for (int i = 0; i < 4; i++) {
          *(LDSP u32x4*)(fa + sb + i * 4096) = u32x4{pk2(c[0], c[1]), pk2(c[2], c[3]), pk2(c[4], c[5]), pk2(c[6], c[7])};
          *(LDSP u32x4*)(fa + sb + i * 4096 + 16384) = u32x4{pk2(sn[0], sn[1]), pk2(sn[2], sn[3]), pk2(sn[4], sn[5]), pk2(sn[6], sn[7])};
#pragma unroll
          for (int e = 0; e < 8; e++) { const float t = c[e] * rc[e] - sn[e] * rs[e]; sn[e] = sn[e] * rc[e] + c[e] * rs[e]; c[e] = t; }
        }
      };
      stage_x(0, 0); stage_x(1, STG); stage_x(2, 2 * STG);
      gen(0);
      *(LDSP float*)(lds + CS_OFF + f * 4) = (f == 0 && cb == 14) ? 512.f : 0.f;
      WAIT_V(8); WAIT_L(0); BAR();
      gen(STG); xstats(0);
      WAIT_V(4); WAIT_L(0); BAR();
      unsigned sb = STG, fb = 0;
#pragma unroll 1
      for (int s = 1; s < 5; s++) {
        const unsigned nb = (sb == 2 * STG) ? 0 : sb + STG;
        stage_x(s + 2, fb);
        gen(nb); xstats(sb);
        WAIT_V(4); WAIT_L(0); BAR();
        fb = sb; sb = nb;
      }
      stage_x(7, STG);
      wt_load();
      gen(0); xstats(2 * STG);
      WAIT_V(22); WAIT_L(0); BAR();
      gen(STG); xstats(0);
      WAIT_V(18); WAIT_L(0); BAR();
      xstats(STG);
    }
#pragma unroll
    for (int i = 0; i < 4; i++) {
      float a = sx[i][0] + sx[i][1], b = sq[i][0] + sq[i][1];
      a += __shfl_xor(a, 1); b += __shfl_xor(b, 1);
      a += __shfl_xor(a, 2); b += __shfl_xor(b, 2);
      a += __shfl_xor(a, 4); b += __shfl_xor(b, 4);
      const float mu = a * (1.f / 512.f);
      const float var = fmaxf(b * (1.f / 512.f) - mu * mu, 0.f);
      const float sd = rbf(__builtin_sqrtf(rbf(rbf(var) + 1e-5f)));
      if ((lane & 7) == 0) *(LDSP f32x2*)(lds + ST_OFF + (32 * hw + 8 * i + (lane >> 3)) * 8) = f32x2{sd, rbf(mu)};
    }
    WAIT_L(0); STAMP(lds + DG1, 17); BAR();
    const rsrc_t rWT = mk_rsrc(ws + OFF_WT, WT_BYTES);
    u32x4 o0, o1;
#pragma unroll
    for (int j = 0; j < 4; j++) { o0[j] = a_r[j] | ((unsigned)a_i[j] << 16); o1[j] = a_r[j + 4] | ((unsigned)a_i[j + 4] << 16); }
    st128<0>(o0, rWT, f * 8192, bid * 32);
    st128<0>(o1, rWT, f * 8192 + 16, bid * 32);
    if (f < 8) __builtin_amdgcn_raw_buffer_store_b32(n_r | ((unsigned)n_i << 16), rWT, 256 * 8192 + f * 4, bid * 32, 0);
    STAMP(lds + DG1, 19);
  }
  FLUSH(lds + DG1, 0);
}

constexpr unsigned UROW = 272;
constexpr unsigned KF_LDS = 16384 + 1024, DGF = 16384;

DI void col_fft(char* ws, int f, int w, unsigned lane, unsigned lds) {
  const rsrc_t rT = mk_rsrc(ws + OFF_T, T_BYTES), rWT = mk_rsrc(ws + OFF_WT, WT_BYTES), rZ = mk_rsrc(ws + OFF_Z, T_BYTES);
  const unsigned c = lane & 15, q = lane >> 4;
  STAMP(lds + DGF, 0);
  u32x4 xa[2][4];
#pragma unroll
  for (int j = 0; j < 4; j++)
#pragma unroll
    for (int ta = 0; ta < 2; ta++) xa[ta][j] = __builtin_amdgcn_raw_buffer_load_b128(rT, q * 512 + c * 16 + j * 2048 + ta * 256, f * 8192, 0);
  u32x4 wv[2];
#pragma unroll
  for (int t = 0; t < 2; t++) wv[t] = __builtin_amdgcn_raw_buffer_load_b128(rWT, lane * 32 + t * 16, f * 8192 + w * 2048, 0);
  const unsigned k1 = 16 * w + c;
  u32x4 bre[4], bim[4];
#pragma unroll
  for (int j = 0; j < 4; j++)
#pragma unroll
    for (int e = 0; e < 4; e++) {
      const float a = (float)(k1 * (16 * j + 4 * q + e)) * (1.f / 64.f);
      const float co = cosr(a), si = sinr(a);
      bre[j][e] = pk2(co, si); bim[j][e] = pk2(-si, co);
    }
  u32x4 c32[2], s32[2], n32[2];
  {
    float co[4], si[4];
#pragma unroll
    for (int e = 0; e < 4; e++) { const float a = (float)(c * (4 * q + e)) * (1.f / 32.f); co[e] = cosr(a); si[e] = sinr(a); }
    const unsigned sg = (c & 1) ? 0x80008000u : 0u;
    const unsigned c01 = pk2(co[0], co[1]), c23 = pk2(co[2], co[3]), s01 = pk2(si[0], si[1]), s23 = pk2(si[2], si[3]);
    c32[0] = u32x4{c01, c23, c01 ^ sg, c23 ^ sg};
    s32[0] = u32x4{s01, s23, s01 ^ sg, s23 ^ sg};
    c32[1] = c32[0] ^ 0x80000000u; s32[1] = s32[0] ^ 0x80000000u;
    n32[0] = s32[0] ^ 0x80008000u; n32[1] = s32[1] ^ 0x80008000u;
  }
  float twc[2][4], tws[2][4];
#pragma unroll
  for (int ta = 0; ta < 2; ta++)
#pragma unroll
    for (int e = 0; e < 4; e++) { const float a = (float)(k1 * (16 * ta + 4 * q + e)) * (1.f / 2048.f); twc[ta][e] = cosr(a); tws[ta][e] = sinr(a); }
  const f32x4 z4 = {0.f, 0.f, 0.f, 0.f};
  STAMP(lds + DGF, 1);
  f32x4 yr[2] = {z4, z4}, yi[2] = {z4, z4};
#pragma unroll
  for (int j = 0; j < 4; j++)
#pragma unroll
    for (int ta = 0; ta < 2; ta++) { yr[ta] = mfma16(xa[ta][j], bre[j], yr[ta]); yi[ta] = mfma16(xa[ta][j], bim[j], yi[ta]); }
  u32x4 pr, pi;
#pragma unroll
  for (int ta = 0; ta < 2; ta++) {
    float a[4], b[4];
#pragma unroll
    for (int e = 0; e < 4; e++) { a[e] = yr[ta][e] * twc[ta][e] + yi[ta][e] * tws[ta][e]; b[e] = yi[ta][e] * twc[ta][e] - yr[ta][e] * tws[ta][e]; }
    pr[2 * ta] = pk2(a[0], a[1]); pr[2 * ta + 1] = pk2(a[2], a[3]); pi[2 * ta] = pk2(b[0], b[1]); pi[2 * ta + 1] = pk2(b[2], b[3]);
  }
  f32x4 xr[2], xi[2];
#pragma unroll
  for (int t = 0; t < 2; t++) {
    xr[t] = mfma16(c32[t], pr, z4); xr[t] = mfma16(s32[t], pi, xr[t]);
    xi[t] = mfma16(c32[t], pi, z4); xi[t] = mfma16(n32[t], pr, xi[t]);
  }
  STAMP(lds + DGF, 2);
  float fr[2][4], fi[2][4];
  if (f != 0) {
    const float sc = 1.f / 524288.f;
#pragma unroll
    for (int t = 0; t < 2; t++)
#pragma unroll
      for (int e = 0; e < 4; e++) {
        const float a = bflo(wv[t][e]) * sc, b = bfhi(wv[t][e]) * sc;
        fr[t][e] = xr[t][e] * a - xi[t][e] * b; fi[t][e] = xr[t][e] * b + xi[t][e] * a;
      }
  } else {
#pragma unroll
    for (int t = 0; t < 2; t++)
#pragma unroll
      for (int e = 0; e < 4; e++) *(LDSP f32x2*)(lds + (k1 + 64 * (16 * t + 4 * q + e)) * 8) = f32x2{xr[t][e], xi[t][e]};
    WAIT_L(0); BAR();
    const float sc = 0.25f / 1048576.f;
#pragma unroll
    for (int t = 0; t < 2; t++)
#pragma unroll
      for (int e = 0; e < 4; e++) {
        const unsigned k = k1 + 64 * (16 * t + 4 * q + e), kn = (2048 - k) & 2047;
        const unsigned m1 = kn & 63, m2 = kn >> 6;
        const unsigned pos = ((m1 >> 4) * 64 + ((m2 >> 2) & 3) * 16 + (m1 & 15)) * 32 + ((m2 >> 4) * 4 + (m2 & 3)) * 4;
        const unsigned d0 = wv[t][e], d0n = __builtin_amdgcn_raw_buffer_load_b32(rWT, pos, 0, 0);
        const unsigned d1 = __builtin_amdgcn_raw_buffer_load_b32(rWT, lane * 32 + t * 16 + e * 4, 256 * 8192 + w * 2048, 0);
        const unsigned d1n = __builtin_amdgcn_raw_buffer_load_b32(rWT, pos, 256 * 8192, 0);
        const f32x2 pn = *(LDSP f32x2*)(lds + kn * 8);
        const float ar = bflo(d0) + bflo(d0n), ai = bfhi(d0) - bfhi(d0n), br = bflo(d1) + bflo(d1n), bi = bfhi(d1) - bfhi(d1n);
        const float war = (ar + br) * sc, wai = (ai + bi) * sc, wbr = (ar - br) * sc, wbi = (ai - bi) * sc;
        fr[t][e] = xr[t][e] * war - xi[t][e] * wai + pn[0] * wbr + pn[1] * wbi;
        fi[t][e] = xr[t][e] * wai + xi[t][e] * war + pn[0] * wbi - pn[1] * wbr;
      }
    WAIT_L(0); BAR();
  }
#pragma unroll
  for (int t = 0; t < 2; t++) {
    pr[2 * t] = pk2(fr[t][0], fr[t][1]); pr[2 * t + 1] = pk2(fr[t][2], fr[t][3]);
    pi[2 * t] = pk2(fi[t][0], fi[t][1]); pi[2 * t + 1] = pk2(fi[t][2], fi[t][3]);
  }
#pragma unroll
  for (int ta = 0; ta < 2; ta++) {
    f32x4 ur = mfma16(c32[ta], pr, z4); ur = mfma16(n32[ta], pi, ur);
    f32x4 ui = mfma16(c32[ta], pi, z4); ui = mfma16(s32[ta], pr, ui);
#pragma unroll
    for (int e = 0; e < 4; e++) {
      const float a = ur[e] * twc[ta][e] - ui[e] * tws[ta][e], b = ui[e] * twc[ta][e] + ur[e] * tws[ta][e];
      *(LDSP unsigned*)(lds + (16 * ta + 4 * q + e) * UROW + k1 * 4) = pk2(a, -b);
    }
  }
  STAMP(lds + DGF, 3);
  WAIT_L(0); BAR();
  STAMP(lds + DGF, 4);
#pragma unroll
  for (int ta = 0; ta < 2; ta++) {
    f32x4 zr = z4, zi = z4;
#pragma unroll
    for (int j = 0; j < 4; j++) {
      const u32x4 u = lds128(lds + (16 * ta + c) * UROW + (16 * j + 4 * q) * 4);
      zr = mfma16(bre[j], u, zr); zi = mfma16(bim[j], u, zi);
    }
    const u32x4 o = {pk2(zr[0], zi[0]), pk2(zr[1], zi[1]), pk2(zr[2], zi[2]), pk2(zr[3], zi[3])};
    st128<AUX_WT>(o, rZ, q * 512 + c * 16 + ta * 256, f * 8192 + w * 2048);
  }
  STAMP(lds + DGF, 5);
  FLUSH(lds + DGF, 1);
}

__global__ __launch_bounds__(256) void kf(char* __restrict__ ws) {
  __shared__ __attribute__((aligned(16))) char lds_[KF_LDS];
  col_fft(ws, blockIdx.x, __builtin_amdgcn_readfirstlane(threadIdx.x >> 6), threadIdx.x & 63, (unsigned)(size_t)(LDSP char*)lds_);
}

constexpr unsigned K2_LDS = 131072 + 1024, DG2 = 131072;
constexpr int PF = 4;

__global__ __launch_bounds__(512, 2) void k2(const void* __restrict__ px, const void* __restrict__ pwd, char* __restrict__ ws, void* __restrict__ pout) {
  extern __shared__ __attribute__((aligned(16))) char lds_[];
  const unsigned lds = (unsigned)(size_t)(LDSP char*)lds_;
  const int bid = blockIdx.x, xcd = bid & 7, idx = bid >> 3;
  const int mt = xcd * 4 + (idx & 3), nb = idx >> 2;
  const int rb = mt >> 1, half = mt & 1;
  const int wave = __builtin_amdgcn_readfirstlane(threadIdx.x >> 6);
  const unsigned lane = threadIdx.x & 63, hh = lane >> 5, rp = (lane >> 4) & 1, c = lane & 15, l32 = lane & 31;
  STAMP(lds + DG2, 0);
  f32x16 acc[2][2];
#pragma unroll
  for (int u = 0; u < 2; u++)
#pragma unroll
    for (int v = 0; v < 2; v++)
#pragma unroll
      for (int i = 0; i < 16; i++) acc[u][v][i] = 0.f;
  if (wave < 4) {
    const rsrc_t rH = mk_rsrc(ws + OFF_H, H_BYTES), rW = mk_rsrc(pwd, W1_BYTES);
    const unsigned av = rp * 2 * 112 * 1024 + (hh * 32 + 16 * half + c) * 16;
    const unsigned bv = hh * 4096 + l32 * 4;
    u32x4 a[PF][2]; unsigned b[PF][8];
    auto ld = [&](int slot, int ks) {
#pragma unroll
      for (int u = 0; u < 2; u++) a[slot][u] = __builtin_amdgcn_raw_buffer_load_b128(rH, av, ((4 * rb + u) * 112 + ks) * 1024, 0);
#pragma unroll
      for (int j = 0; j < 8; j++) b[slot][j] = __builtin_amdgcn_raw_buffer_load_b32(rW, bv + (j & 3) * 1024, (16 * ks + 8 * (j >> 2)) * 1024 + nb * 128, 0);
    };
    auto mm = [&](int slot) {
      u32x4 be, bo;
#pragma unroll
      for (int j = 0; j < 4; j++) {
        be[j] = __builtin_amdgcn_perm(b[slot][2 * j + 1], b[slot][2 * j], 0x05040100u);
        bo[j] = __builtin_amdgcn_perm(b[slot][2 * j + 1], b[slot][2 * j], 0x07060302u);
      }
#pragma unroll
      for (int u = 0; u < 2; u++) { acc[u][0] = mfma32(a[slot][u], be, acc[u][0]); acc[u][1] = mfma32(a[slot][u], bo, acc[u][1]); }
    };
    const int ks0 = 28 * wave;
#pragma unroll
    for (int i = 0; i < PF; i++) ld(i, ks0 + i);
#pragma unroll 1
    for (int k = PF; k < 28; k += PF) {
      STAMP(lds + DG2, k / PF);
#pragma unroll
      for (int i = 0; i < PF; i++) { mm(i); __builtin_amdgcn_sched_barrier(0); ld(i, ks0 + k + i); __builtin_amdgcn_sched_barrier(0); }
    }
#pragma unroll
    for (int i = 0; i < PF; i++) mm(i);
  } else {
    const int wz = wave - 4;
    const rsrc_t rZ = mk_rsrc(ws + OFF_Z, T_BYTES);
    u32x2 z[8][4];
    const unsigned zv = hh * 4 * 8192 + (16 * half + c) * 16 + 8 * rp;
#pragma unroll
    for (int ks = 0; ks < 8; ks++)
#pragma unroll
      for (int j = 0; j < 4; j++) z[ks][j] = __builtin_amdgcn_raw_buffer_load_b64(rZ, zv, (64 * wz + 8 * ks + j) * 8192 + rb * 512, 0);
    const float d0 = (float)(64 * nb + 2 * l32) * (1.f / 512.f), d1 = d0 + (1.f / 512.f);
    const float fb = (float)(64 * wz + 4 * hh);
#pragma unroll
    for (int ks = 0; ks < 8; ks++) {
      u32x4 be, bo;
#pragma unroll
      for (int j = 0; j < 4; j++) {
        const float ff = fb + (float)(8 * ks + j);
        be[j] = pk2(cosr(ff * d0), sinr(ff * d0)); bo[j] = pk2(cosr(ff * d1), sinr(ff * d1));
      }
      if (ks == 0 && wz == 0 && hh == 0) { be[0] = 0xbf803f80u; bo[0] = 0x3f803f80u; }
#pragma unroll
      for (int u = 0; u < 2; u++) {
        const u32x4 a = {z[ks][0][u], z[ks][1][u], z[ks][2][u], z[ks][3][u]};
        acc[u][0] = mfma32(a, be, acc[u][0]); acc[u][1] = mfma32(a, bo, acc[u][1]);
      }
    }
  }
  STAMP(lds + DG2, 10);
  const int u0 = wave >> 2, g0 = wave & 3;
  const rsrc_t rX = mk_rsrc(px, X_BYTES), rO = mk_rsrc(pout, X_BYTES);
  const unsigned so = (128 * rb + 32 * (2 * (g0 >> 1) + u0) + 16 * half + 8 * (g0 & 1)) * 1024 + nb * 128;
  const unsigned ov = hh * 4096 + l32 * 4;
  unsigned xr[4];
#pragma unroll
  for (int e = 0; e < 4; e++) xr[e] = __builtin_amdgcn_raw_buffer_load_b32(rX, ov + e * 1024, so, 0);
#pragma unroll
  for (int u = 0; u < 2; u++)
#pragma unroll
    for (int v = 0; v < 2; v++)
#pragma unroll
      for (int g = 0; g < 4; g++)
        *(LDSP f32x4*)(lds + wave * 16384 + ((u * 2 + v) * 4 + g) * 1024 + lane * 16) = f32x4{acc[u][v][4 * g], acc[u][v][4 * g + 1], acc[u][v][4 * g + 2], acc[u][v][4 * g + 3]};
  WAIT_L(0); STAMP(lds + DG2, 11); BAR(); STAMP(lds + DG2, 12);
  f32x4 s0 = {0.f, 0.f, 0.f, 0.f}, s1 = s0;
#pragma unroll
  for (int pw = 0; pw < 8; pw++) {
    s0 += *(LDSP f32x4*)(lds + pw * 16384 + ((u0 * 2 + 0) * 4 + g0) * 1024 + lane * 16);
    s1 += *(LDSP f32x4*)(lds + pw * 16384 + ((u0 * 2 + 1) * 4 + g0) * 1024 + lane * 16);
  }
#pragma unroll
  for (int e = 0; e < 4; e++) __builtin_amdgcn_raw_buffer_store_b32(pk2(bflo(xr[e]) + s0[e], bfhi(xr[e]) + s1[e]), rO, ov + e * 1024, so, 0);
  STAMP(lds + DG2, 13);
  FLUSH(lds + DG2, 2);
}

__global__ void kdiag(const char* ws) {
  const unsigned* d = (const unsigned*)(ws + DIAG_OFF);
  __shared__ unsigned mn[16], mx[16];
  const int sel[16][3] = {{0,0,0},{0,0,1},{0,0,3},{0,0,9},{0,0,17},{0,0,31},{1,0,0},{1,0,1},{1,0,3},{1,0,31},{2,0,0},{2,0,1},{2,0,2},{2,0,10},{2,4,10},{2,0,31}};
  if (threadIdx.x < 16) { mn[threadIdx.x] = 0xffffffffu; mx[threadIdx.x] = 0; }
  __syncthreads();
  unsigned t0 = d[0];
  for (int i = 0; i < 16; i++) {
    unsigned v = d[((sel[i][0] * 256 + threadIdx.x) * 8 + sel[i][1]) * 32 + sel[i][2]] - t0 + 1000;
    atomicMin(&mn[i], v); atomicMax(&mx[i], v);
  }
  __syncthreads();
  if (threadIdx.x == 0) {
    unsigned b = mn[0];
    printf("S %u/%u %u/%u %u/%u %u/%u %u/%u %u/%u | %u/%u %u/%u %u/%u %u/%u | %u/%u %u/%u %u/%u %u/%u %u/%u %u/%u\n",
      mn[0]-b,mx[0]-b,mn[1]-b,mx[1]-b,mn[2]-b,mx[2]-b,mn[3]-b,mx[3]-b,mn[4]-b,mx[4]-b,mn[5]-b,mx[5]-b,mn[6]-b,mx[6]-b,mn[7]-b,mx[7]-b,
      mn[8]-b,mx[8]-b,mn[9]-b,mx[9]-b,mn[10]-b,mx[10]-b,mn[11]-b,mx[11]-b,mn[12]-b,mx[12]-b,mn[13]-b,mx[13]-b,mn[14]-b,mx[14]-b,mn[15]-b,mx[15]-b);
  }
}
extern "C" void kernel_launch(void* const* d_in, const int* in_sizes, int n_in, void* d_out, int out_size, void* d_ws, size_t ws_size,
                              hipStream_t stream) {
  (void)in_sizes; (void)n_in; (void)out_size; (void)ws_size;
  (void)hipFuncSetAttribute((const void*)k1, hipFuncAttributeMaxDynamicSharedMemorySize, K1_LDS);
  (void)hipFuncSetAttribute((const void*)k2, hipFuncAttributeMaxDynamicSharedMemorySize, K2_LDS);
  k1<<<256, 512, K1_LDS, stream>>>(d_in[0], d_in[4], d_in[5], d_in[1], d_in[2], (char*)d_ws);
  kf<<<256, 256, 0, stream>>>((char*)d_ws);
  k2<<<256, 512, K2_LDS, stream>>>(d_in[0], d_in[6], (char*)d_ws, d_out);
  kdiag<<<1, 256, 0, stream>>>((char*)d_ws);
}
```

```cpp
#include <hip/hip_runtime.h>
#include <hip/hip_bf16.h>
#pragma clang diagnostic ignored "-Wint-to-pointer-cast"

typedef __bf16 bf16x8 __attribute__((ext_vector_type(8)));
typedef __bf16 bf16x2 __attribute__((ext_vector_type(2)));
typedef short s16x4 __attribute__((ext_vector_type(4)));
typedef float f32x4 __attribute__((ext_vector_type(4)));
typedef float f32x2 __attribute__((ext_vector_type(2)));
typedef unsigned u32x4 __attribute__((ext_vector_type(4)));
typedef unsigned u32x2 __attribute__((ext_vector_type(2)));
#define LDSP __attribute__((address_space(3)))
#define DI __device__ __forceinline__
typedef __amdgpu_buffer_rsrc_t rsrc_t;

constexpr unsigned X_BYTES = 2048u * 512u * 2u;
constexpr unsigned W1_BYTES = 512u * 1792u * 2u;
constexpr unsigned WF_BYTES = 2048u * 257u * 2u;
constexpr unsigned OFF_T = 2048u * 1792u * 2u, OFF_Z = OFF_T + 256u * 8192u, OFF_WT = OFF_Z + 256u * 8192u, OFF_SY = OFF_WT + 260u * 8192u;
constexpr unsigned SY_E = OFF_SY, SY_FH = OFF_SY + 1024, SY_FT = OFF_SY + 3072, SY_FW = OFF_SY + 4096, SY_FZ = OFF_SY + 5120,
                   SY_RG = OFF_SY + 8192, SY_CG = OFF_SY + 24576, WS_BYTES = SY_CG + 14u * 8192u;
constexpr unsigned OOB = 0x40000000u;
constexpr int SC1 = 16;
#define SPINV (1 << 20)
constexpr int SPIN = SPINV;
constexpr unsigned STG = 49152, L_U = 3 * STG, L_ST = L_U + 8704, L_CS = L_ST + 2048, L_CP = L_CS + 1024, L_CN = L_CP + 1024, L_DG = L_CN + 512,
                   LDS_BYTES = 163840;
constexpr unsigned CN_TAG = 0, CN_H = 4, CN_Z = 8, CN_G = 12, CN_B = 16;
constexpr unsigned UROW = 272;

DI rsrc_t mk_rsrc(const void* p, unsigned bytes) { return __builtin_amdgcn_make_buffer_rsrc((void*)p, 0, bytes, 0x00020000); }
DI unsigned pk2(float a, float b) { bf16x2 v = {(__bf16)a, (__bf16)b}; return __builtin_bit_cast(unsigned, v); }
DI float bflo(unsigned u) { return __builtin_bit_cast(float, u << 16); }
DI float bfhi(unsigned u) { return __builtin_bit_cast(float, u & 0xffff0000u); }
DI float rbf(float a) { return (float)(__bf16)a; }
DI float asf(unsigned u) { return __builtin_bit_cast(float, u); }
DI unsigned asu(float f) { return __builtin_bit_cast(unsigned, f); }
DI f32x4 mfma(u32x4 a, u32x4 b, f32x4 c) {
  return __builtin_amdgcn_mfma_f32_16x16x32_bf16(__builtin_bit_cast(bf16x8, a), __builtin_bit_cast(bf16x8, b), c, 0, 0, 0);
}
DI float cosr(float rev) { return __builtin_amdgcn_cosf(rev); }
DI float sinr(float rev) { return __builtin_amdgcn_sinf(rev); }
template <int N> DI void wait_v() { asm volatile("s_waitcnt vmcnt(%0)" :: "i"(N) : "memory"); }
#define WAIT_L0() asm volatile("s_waitcnt lgkmcnt(0)" ::: "memory")
#define BAR() do { asm volatile("" ::: "memory"); __builtin_amdgcn_s_barrier(); asm volatile("" ::: "memory"); } while (0)
#define SB() __builtin_amdgcn_sched_barrier(0)
template <class T> DI void pin(T& v) { asm volatile("" : "+v"(v)); }
DI void dma16(rsrc_t r, unsigned ldsaddr, unsigned voff, unsigned soff) {
  asm volatile("s_mov_b32 m0, %0\n\ts_nop 0\n\tbuffer_load_dwordx4 %1, %2, %3 offen lds" :: "s"(ldsaddr), "v"(voff), "s"(r), "s"(soff) : "memory");
}
DI u32x4 ald128(rsrc_t r, unsigned v, unsigned s) { u32x4 o; asm volatile("buffer_load_dwordx4 %0, %1, %2, %3 offen" : "=v"(o) : "v"(v), "s"(r), "s"(s) : "memory"); return o; }
DI u32x2 ald64s(rsrc_t r, unsigned v, unsigned s) { u32x2 o; asm volatile("buffer_load_dwordx2 %0, %1, %2, %3 offen sc1" : "=v"(o) : "v"(v), "s"(r), "s"(s) : "memory"); return o; }
DI unsigned ald32s(rsrc_t r, unsigned v, unsigned s) { unsigned o; asm volatile("buffer_load_dword %0, %1, %2, %3 offen sc1" : "=v"(o) : "v"(v), "s"(r), "s"(s) : "memory"); return o; }
DI unsigned ald16(rsrc_t r, unsigned v, unsigned s) { unsigned o; asm volatile("buffer_load_ushort %0, %1, %2, %3 offen" : "=v"(o) : "v"(v), "s"(r), "s"(s) : "memory"); return o; }
DI u32x4 ld128s(rsrc_t r, unsigned v, unsigned s) { return __builtin_amdgcn_raw_buffer_load_b128(r, v, s, SC1); }
template <int AUX> DI void st128(u32x4 v, rsrc_t r, unsigned voff) { __builtin_amdgcn_raw_buffer_store_b128(v, r, voff, 0, AUX); }
DI void st64s(u32x2 v, rsrc_t r, unsigned voff) { __builtin_amdgcn_raw_buffer_store_b64(v, r, voff, 0, SC1); }
DI void st32s(unsigned v, rsrc_t r, unsigned voff) { __builtin_amdgcn_raw_buffer_store_b32(v, r, voff, 0, SC1); }
DI u32x4 lds128(unsigned a) { return *(LDSP u32x4*)a; }
DI f32x4 ldsf4(unsigned a) { return *(LDSP f32x4*)a; }
DI u32x2 ldstr(unsigned a) { return __builtin_bit_cast(u32x2, __builtin_amdgcn_ds_read_tr16_b64_v4i16((LDSP s16x4*)a)); }
template <int D> DI u32x4 ldstr2(unsigned a) { const u32x2 lo = ldstr(a), hi = ldstr(a + D); return u32x4{lo[0], lo[1], hi[0], hi[1]}; }
template <int C> DI float dpp_add(float v) { return v + asf(__builtin_amdgcn_update_dpp(0, asu(v), C, 0xf, 0xf, true)); }
DI float swap16_add(float a, float b) { const auto r = __builtin_amdgcn_permlane16_swap(asu(a), asu(b), false, false); return asf(r[0]) + asf(r[1]); }
DI float swap32_add(float a, float b) { const auto r = __builtin_amdgcn_permlane32_swap(asu(a), asu(b), false, false); return asf(r[0]) + asf(r[1]); }
DI float sum32(float v) { v = dpp_add<0xB1>(v); v = dpp_add<0x4E>(v); v = dpp_add<0x141>(v); v = dpp_add<0x128>(v); return swap16_add(v, v); }
DI float add1(float a, float b) { float o; asm("v_add_f32 %0, %1, %2" : "=v"(o) : "v"(a), "v"(b)); return o; }
DI float fma1(float a, float b, float c) { float o; asm("v_fma_f32 %0, %1, %2, %3" : "=v"(o) : "v"(a), "v"(b), "v"(c)); return o; }
DI float fms1(float a, float b, float c) { float o; asm("v_fma_f32 %0, %1, %2, -%3" : "=v"(o) : "v"(a), "v"(b), "v"(c)); return o; }
DI void poll(rsrc_t r, unsigned voff, bool need, unsigned tag) {
#pragma unroll 1
  for (int it = 0; it < SPIN; it++) {
    const unsigned v = __builtin_amdgcn_raw_buffer_load_b32(r, voff, 0, SC1);
    if (__all(!need || v == tag)) break;
    __builtin_amdgcn_s_sleep(1);
    asm volatile("" ::: "memory");
  }
  asm volatile("" ::: "memory");
}
DI void lds_wait(unsigned a, unsigned n) {
#pragma unroll 1
  for (int it = 0; it < SPIN; it++) { if (*(volatile LDSP unsigned*)a >= n) break; }
  asm volatile("" ::: "memory");
}
DI unsigned lds_arrive(unsigned a, unsigned lane) {
  unsigned old = 0;
  if (lane == 0) old = __hip_atomic_fetch_add((LDSP unsigned*)a, 1u, __ATOMIC_RELAXED, __HIP_MEMORY_SCOPE_WORKGROUP);
  return old;
}
DI unsigned bufaddr(int b, int pair) { return (b < 3 ? 2 * STG : b < 6 ? 0 : STG) + (b % 3) * 4096 + pair * 12288; }

#define STAMP(i)
#define FLUSH()

__global__ __launch_bounds__(512, 2) void fused(const void* __restrict__ px, const void* __restrict__ pwr, const void* __restrict__ pwi,
                                                const void* __restrict__ pwg, const void* __restrict__ pwu, const void* __restrict__ pwd,
                                                char* ws, void* __restrict__ pout) {
  extern __shared__ __attribute__((aligned(16))) char lds_[];
  const unsigned lds = (unsigned)(size_t)(LDSP char*)lds_;
  asm volatile("" :: "s"(px), "s"(pwr), "s"(pwi), "s"(pwg), "s"(pwu), "s"(ws));
  const int bid = blockIdx.x, xcd = bid & 7, idx = bid >> 3;
  const int rb = (xcd & 1) * 8 + (idx & 7), cb = (xcd >> 1) * 4 + (idx >> 3);
  const int rb2 = (xcd & 1) * 8 + (xcd >> 1) * 2 + (idx & 1), half = (idx >> 1) & 1, nb = idx >> 2;
  const bool dft = cb >= 14;
  const int wave = __builtin_amdgcn_readfirstlane(threadIdx.x >> 6), wi = wave & 3;
  const unsigned lane = threadIdx.x & 63, c = lane & 15, q = lane >> 4;
  const rsrc_t rS = mk_rsrc(ws, WS_BYTES);
  const f32x4 z4 = {0.f, 0.f, 0.f, 0.f};
  const unsigned hv = rb2 * (56 * 8192) + half * 1024 + lane * 16;
  const unsigned tb = lds + wi * 12288 + (4 * q + (c >> 2)) * 128 + (((2 * q + (c >> 3)) & 3) << 5) + (c & 3) * 8;
  STAMP(0);

  if (wave < 4) {
    __builtin_amdgcn_s_setprio(2);
    unsigned xa0 = lds + c * 128 + ((q ^ (c >> 1)) << 4), xa1 = xa0 ^ 64;
    const unsigned key = (c >> 2) | ((q & 1) << 2);
    unsigned wa0 = lds + 16384 + (8 * q + (c >> 2)) * 256 + (((2 * wi) ^ key) << 5) + (c & 3) * 8, wa1 = wa0 ^ 32;
    f32x4 acc[8][4];
#pragma unroll
    for (int t = 0; t < 8; t++)
#pragma unroll
      for (int m = 0; m < 4; m++) { acc[t][m] = z4; pin(acc[t][m]); }
    if (wave == 0 && lane < 8) *(LDSP unsigned*)(lds + L_CN + lane * 4) = 0;
    u32x4 W[2][4], X[2][4];
    auto lw = [&](int b, int m0, int ks) {
#pragma unroll
      for (int m = m0; m < m0 + 2; m++) W[b][m] = ldstr2<1024>(((m & 1) ? wa1 : wa0) + ks * 8192 + (m >> 1) * 16384);
    };
    auto lx = [&](int h, int ks) {
#pragma unroll
      for (int t = 0; t < 4; t++) X[h][t] = lds128((ks ? xa1 : xa0) + (h * 4 + t) * 2048);
    };
    auto mm = [&](int b, int h, int t0, int t1) {
#pragma unroll
      for (int t = t0; t < t1; t++)
#pragma unroll
        for (int m = 0; m < 4; m++) acc[h * 4 + t][m] = mfma(W[b][m], X[h][t], acc[h * 4 + t][m]);
#pragma unroll
      for (int i = 0; i < (t1 - t0) * 4; i++) { __builtin_amdgcn_sched_group_barrier(0x008, 1, 0); __builtin_amdgcn_sched_group_barrier(0x102, 1, 0); }
      SB();
    };
    WAIT_L0(); BAR();
    STAMP(1);
    lw(0, 0, 0); lw(0, 2, 0); lx(0, 0);
    int slot = 0;
#pragma unroll 1
    for (int s = 0; s < 8; s++) {
      const unsigned dl = (slot == 2) ? -2 * STG : STG;
      slot = (slot == 2) ? 0 : slot + 1;
      lx(1, 0); lw(1, 0, 1); mm(0, 0, 0, 4);
      lw(1, 2, 1); lx(0, 1); mm(0, 1, 0, 4);
      lx(1, 1); xa0 += dl; xa1 += dl; wa0 += dl; wa1 += dl; mm(1, 0, 0, 3);
      pin(xa0); pin(xa1); pin(wa0); pin(wa1);
      WAIT_L0(); BAR();
      lw(0, 0, 0); mm(1, 0, 3, 4);
      lw(0, 2, 0); lx(0, 0); mm(1, 1, 0, 4);
    }
    STAMP(2);
    const unsigned tag = *(LDSP unsigned*)(lds + L_CN + CN_TAG);
    f32x4 cs[4];
#pragma unroll
    for (int m = 0; m < 4; m++) cs[m] = ldsf4(lds + L_CS + ((m >> 1) * 128 + 32 * wi + 16 * (m & 1) + 4 * q) * 4);
    unsigned flag;
    if (!dft) {
      const unsigned ho = (rb * 56 + cb * 4 + wi) * 8192 + lane * 16;
#pragma unroll
      for (int t = 0; t < 8; t++) {
        const f32x4 st = ldsf4(lds + L_ST + (16 * t + c) * 16);
        u32x4 o;
#pragma unroll
        for (int p = 0; p < 4; p++) {
          const int v = p >> 1, e = (p & 1) * 2;
          const f32x2 g = f32x2{acc[t][v][e], acc[t][v][e + 1]} - st[0] * f32x2{cs[v][e], cs[v][e + 1]};
          const f32x2 u = f32x2{acc[t][2 + v][e], acc[t][2 + v][e + 1]} - st[0] * f32x2{cs[2 + v][e], cs[2 + v][e + 1]};
          const f32x2 y = g * (st[1] + st[2] * (g * g));
          const f32x2 ex = {__builtin_amdgcn_exp2f(y[0]), __builtin_amdgcn_exp2f(y[1])};
          const f32x2 d = st[3] + st[3] * ex;
          const f32x2 h = (g * u) * f32x2{__builtin_amdgcn_rcpf(d[0]), __builtin_amdgcn_rcpf(d[1])};
          o[p] = pk2(h[0], h[1]);
        }
        st128<SC1>(o, rS, ho + t * 1024);
      }
      flag = SY_FH + rb * 128 + cb * 4;
    } else {
      const unsigned to = OFF_T + ((cb - 14) * 128 + 32 * wi + 4 * q) * 8192 + rb * 512 + c * 16;
      f32x2 st[8];
#pragma unroll
      for (int t = 0; t < 8; t++) st[t] = *(LDSP f32x2*)(lds + L_ST + (16 * t + c) * 16);
#pragma unroll
      for (int v = 0; v < 2; v++)
#pragma unroll
        for (int e = 0; e < 4; e++)
#pragma unroll
          for (int th = 0; th < 2; th++) {
            u32x4 o;
#pragma unroll
            for (int r = 0; r < 4; r++) { const int t = 2 * r + th; o[r] = pk2(st[t][1] * (acc[t][v][e] - st[t][0] * cs[v][e]), st[t][1] * acc[t][2 + v][e]); }
            st128<SC1>(o, rS, to + (16 * v + e) * 8192 + th * 256);
          }
      flag = SY_FT + (cb - 14) * 128 + rb * 4;
    }
    STAMP(3);
    __builtin_amdgcn_s_setprio(0);
    u32x4 G[4][4];
    const float d0 = (float)(64 * nb + c) * (1.f / 512.f);
    auto gen_g = [&](int zz) {
      const int zs = 4 * wi + ((zz + nb) & 3);
      const float f0 = (float)(16 * zs + 4 * q);
#pragma unroll
      for (int v = 0; v < 4; v++)
#pragma unroll
        for (int j = 0; j < 4; j++) { const float a = (f0 + (float)j) * (d0 + (float)v * (1.f / 32.f)); G[zz][v][j] = pk2(cosr(a), sinr(a)); }
      if (zs == 0) {
#pragma unroll
        for (int v = 0; v < 4; v++) G[zz][v][0] = q ? G[zz][v][0] : (c & 1) ? 0x3f803f80u : 0xbf803f80u;
      }
#pragma unroll
      for (int v = 0; v < 4; v++) pin(G[zz][v]);
    };
    gen_g(0);
    wait_v<0>();
    { const unsigned old = lds_arrive(lds + L_CN + CN_H, lane); st32s(tag, rS, (lane == 0 && old == 3) ? flag : OOB); }
    STAMP(4);
    gen_g(1); gen_g(2); gen_g(3);
    STAMP(5);
    poll(rS, SY_FH + rb2 * 128 + (lane & 31) * 4, lane >= ((14 * wi) >> 2) && lane <= ((14 * wi + 4) >> 2), tag);
    STAMP(6);
    f32x4 a2[4][4];
    {
      u32x4 A[5][4];
#pragma unroll
      for (int j = 0; j < 5; j++) {
        const int S = 14 * wi + (j + nb) % 5;
#pragma unroll
        for (int r = 0; r < 4; r++) A[j][r] = ld128s(rS, hv + r * 2048, S * 8192);
      }
      lds_wait(lds + L_CN + CN_B + wi * 4, 1);
#pragma unroll
      for (int j = 0; j < 5; j++) {
        u32x4 B[4];
#pragma unroll
        for (int v = 0; v < 4; v++) B[v] = ldstr2<2048>((tb ^ (v << 5)) + bufaddr((5 + j) % 9, 0));
#pragma unroll
        for (int r = 0; r < 4; r++)
#pragma unroll
          for (int v = 0; v < 4; v++) a2[r][v] = mfma(B[v], A[j][r], j ? a2[r][v] : z4);
      }
    }
    STAMP(7);
    poll(rS, SY_FZ + (64 * wi + lane) * 4, true, tag);
    STAMP(8);
    {
      u32x4 Z[4][4];
      const unsigned zv = OFF_Z + 4 * q * 8192 + rb2 * 512 + (16 * half + c) * 16;
#pragma unroll
      for (int zz = 0; zz < 4; zz++)
#pragma unroll
        for (int j = 0; j < 4; j++) Z[zz][j] = ld128s(rS, zv, (16 * (4 * wi + ((zz + nb) & 3)) + j) * 8192);
      SB();
#pragma unroll
      for (int zz = 0; zz < 4; zz++)
#pragma unroll
        for (int r = 0; r < 4; r++) {
          const u32x4 zf = {Z[zz][0][r], Z[zz][1][r], Z[zz][2][r], Z[zz][3][r]};
#pragma unroll
          for (int v = 0; v < 4; v++) a2[r][v] = mfma(G[zz][v], zf, a2[r][v]);
        }
    }
    STAMP(9);
#pragma unroll
    for (int r = 0; r < 4; r++)
#pragma unroll
      for (int v = 0; v < 4; v++) *(LDSP f32x4*)(lds + bufaddr(5 + r, wi) + v * 1024 + lane * 16) = a2[r][v];
    WAIT_L0();
    STAMP(10);
    BAR();
    if (bid == 0 && wave == 0) st32s(tag, rS, lane == 0 ? SY_E : OOB);
  } else {
    const rsrc_t rX = mk_rsrc(px, X_BYTES);
    const unsigned xv0 = (lane >> 3) * 1024 + (((lane & 7) ^ q) << 4), xv1 = xv0 ^ 64;
    auto dx = [&](int i, int sx, int slot) { dma16(rX, lds + slot * STG + (4 * wi + i) * 1024, (i & 1) ? xv1 : xv0, (rb * 128 + (4 * wi + i) * 8) * 1024 + sx * 128); };
    unsigned tag;
    unsigned ev; u32x4 xr[2];
    const unsigned row = 128 * rb + 8 * cb + 2 * wi + (lane >> 5);
    auto sa_load = [&]() {
      ev = ald32s(rS, SY_E, 0);
#pragma unroll
      for (int j = 0; j < 2; j++) xr[j] = ald128(rX, row * 1024 + (lane & 31) * 16, j * 512);
    };
    float xf[16], mu;
    auto row_job = [&](int i) {
      if (i == 0) {
        pin(ev); pin(xr[0]); pin(xr[1]);
        tag = __builtin_amdgcn_readfirstlane(ev) + 1;
        *(LDSP unsigned*)(lds + L_CN + CN_TAG) = tag;
#pragma unroll
        for (int e = 0; e < 8; e++) { xf[2 * e] = bflo(xr[e >> 2][e & 3]); xf[2 * e + 1] = bfhi(xr[e >> 2][e & 3]); pin(xf[2 * e]); pin(xf[2 * e + 1]); }
      } else if (i == 1) {
        float a = xf[0], b = xf[1];
#pragma unroll
        for (int e = 1; e < 8; e++) { a = add1(a, xf[2 * e]); b = add1(b, xf[2 * e + 1]); }
        mu = sum32(a + b) * (-1.f / 512.f); pin(mu);
      } else if (i == 2) {
#pragma unroll
        for (int e = 0; e < 16; e++) { xf[e] = add1(xf[e], mu); pin(xf[e]); }
      } else if (i == 3) {
        float a = xf[0] * xf[0], b = xf[1] * xf[1];
#pragma unroll
        for (int e = 1; e < 8; e++) { a = fma1(xf[2 * e], xf[2 * e], a); b = fma1(xf[2 * e + 1], xf[2 * e + 1], b); }
        const float var = sum32(a + b) * (1.f / 512.f);
        const float sd = __builtin_amdgcn_sqrtf(rbf(rbf(var) + 1e-5f));
        st64s(u32x2{pk2(sd, -mu), tag}, rS, (lane & 31) ? OOB : SY_RG + row * 8);
      }
    };
    const rsrc_t rWr = mk_rsrc(pwr, WF_BYTES), rWi = mk_rsrc(pwi, WF_BYTES);
    const int cg = idx & 3, rg = xcd * 8 + (idx >> 2);
    const int k0 = 16 * (rg >> 4) + 8 * (rg & 1) + 1024 * ((rg >> 3) & 1) + 256 * ((rg >> 1) & 3);
    unsigned wr[9], wm[9];
    auto wt_load = [&]() {
#pragma unroll
      for (int j = 0; j < 8; j++) {
        const unsigned so = (k0 + 2 * wi + (j >> 2) + 64 * (j & 3)) * 514 + cg * 128;
        wr[j] = ald16(rWr, lane * 2, so); wm[j] = ald16(rWi, lane * 2, so);
      }
      const unsigned k = k0 + (lane >> 2) + 64 * (lane & 3);
      const unsigned vo = (wi == 0 && lane < 32) ? (cg ? (2048 - k) & 2047 : k) * 514 + (cg == 1 ? 0 : 512) : OOB;
      wr[8] = ald16(rWr, vo, 0); wm[8] = ald16(rWi, vo, 0);
    };
    auto wt_pack = [&]() {
#pragma unroll
      for (int j = 0; j < 9; j++) { pin(wr[j]); pin(wm[j]); wr[j] |= wm[j] << 16; }
#pragma unroll
      for (int d = 0; d < 2; d++) *(LDSP u32x4*)(lds + L_U + lane * 128 + (((2 * wi + d) ^ (lane & 7)) << 4)) = u32x4{wr[4 * d], wr[4 * d + 1], wr[4 * d + 2], wr[4 * d + 3]};
      st32s(wr[8], rS, (wi == 0 && lane < 32) ? OFF_WT + (256 + cg) * 8192 + rg * 128 + lane * 4 : OOB);
    };
    auto wt_store = [&]() {
#pragma unroll
      for (int d = 0; d < 2; d++) {
        const unsigned col = 16 * wi + 8 * d + (lane >> 3);
        st128<SC1>(lds128(lds + L_U + col * 128 + (((lane & 7) ^ (lane >> 3)) << 4)), rS, OFF_WT + (64 * cg + col) * 8192 + rg * 128 + (lane & 7) * 16);
      }
    };
    u32x2 gr, gc[4];
    const unsigned grv = SY_RG + (128 * rb + 32 * wi + (lane & 31)) * 8, gcv = SY_CG + cb * 8192 + (64 * wi + lane) * 8;
    if (!dft) {
      const rsrc_t rG = mk_rsrc(pwg, W1_BYTES), rU = mk_rsrc(pwu, W1_BYTES);
      const unsigned wv0 = q * 3584 + ((c ^ (q << 1)) << 4), wv1 = wv0 ^ 128;
      auto dw = [&](int i, int s, int slot) {
        dma16((i & 1) ? rU : rG, lds + slot * STG + 16384 + (i & 1) * 16384 + (4 * wi + (i >> 1)) * 1024, (i & 4) ? wv1 : wv0, (64 * s + 16 * wi + 4 * (i >> 1)) * 3584 + cb * 256);
      };
      auto stage = [&](int s, auto&& job) {
#pragma unroll
        for (int i = 0; i < 12; i++) { if (i < 4) dx(i, s, s % 3); else dw(i - 4, s, s % 3); job(i); SB(); }
      };
      auto none = [](int) {};
      u32x4 cr[4]; float ca[8];
      auto col_job = [&](int i) {
        if (i < 4) {
          pin(cr[i]);
#pragma unroll
          for (int e = 0; e < 4; e++) {
            ca[2 * e] = i ? add1(ca[2 * e], bflo(cr[i][e])) : bflo(cr[i][e]); ca[2 * e + 1] = i ? add1(ca[2 * e + 1], bfhi(cr[i][e])) : bfhi(cr[i][e]);
            pin(ca[2 * e]); pin(ca[2 * e + 1]);
          }
        } else if (i == 4) {
          float b[4];
#pragma unroll
          for (int e = 0; e < 4; e++) b[e] = swap32_add(ca[e], ca[e + 4]);
          const float c0 = dpp_add<0x128>(swap16_add(b[0], b[2])), c1 = dpp_add<0x128>(swap16_add(b[1], b[3]));
          *(LDSP float*)(lds + L_CP + wi * 256 + (8 * (lane & 7) + (lane >> 3)) * 4) = (lane & 8) ? c1 : c0;
        }
      };
      stage(0, none);
#pragma unroll
      for (int i = 0; i < 4; i++) dx(i, 1, 1);
      wait_v<4>(); BAR();
#pragma unroll
      for (int i = 0; i < 8; i++) dw(i, 1, 1);
      stage(2, none);
      sa_load();
#pragma unroll
      for (int j = 0; j < 4; j++) cr[j] = ald128((rb & 2) ? rU : rG, (lane >> 3) * 3584 + (lane & 7) * 16, (128 * (rb >> 2) + 32 * wi + 8 * j) * 3584 + cb * 256 + (rb & 1) * 128);
      wait_v<19>(); BAR();
      stage(3, none); wt_load();
      wait_v<30>(); BAR();
      stage(4, [&](int i) { if (i < 4) row_job(i); else col_job(i - 4); });
      wait_v<13>(); WAIT_L0(); BAR();
      stage(5, [&](int i) {
        if (i == 0) {
          const unsigned a = lds + L_CP + (16 * wi + c) * 4;
          const float v = (*(LDSP float*)a + *(LDSP float*)(a + 256)) + (*(LDSP float*)(a + 512) + *(LDSP float*)(a + 768));
          st64s(u32x2{asu(v), tag}, rS, q ? OOB : SY_CG + cb * 8192 + (rb >> 2) * 2048 + (64 * (rb & 3) + 16 * wi + c) * 8);
        } else if (i == 1) wt_pack();
      });
      wait_v<14>(); WAIT_L0(); BAR();
      stage(6, [&](int i) { if (i == 0) wt_store(); });
      wait_v<14>(); BAR();
      stage(7, none);
      wait_v<12>(); BAR();
      gr = ald64s(rS, grv, 0);
#pragma unroll
      for (int j = 0; j < 4; j++) gc[j] = ald64s(rS, gcv, j * 2048);
    } else {
      const unsigned f0 = (cb - 14) * 128 + (c ^ ((q | ((wi >> 1) << 2)) << 1)) * 8;
      const int kk = 4 * wi + q;
      float c1[8], c0[8], s1[8], s0[8], K[8];
      u32x4 P[4][2];
      const unsigned fa = lds + 16384 + wi * 1024 + lane * 16;
      auto put = [&](int p, int i) {
        if (!(p & 1)) {
          P[i][0] = u32x4{pk2(c1[0], c1[1]), pk2(c1[2], c1[3]), pk2(c1[4], c1[5]), pk2(c1[6], c1[7])};
          P[i][1] = u32x4{pk2(s1[0], s1[1]), pk2(s1[2], s1[3]), pk2(s1[4], s1[5]), pk2(s1[6], s1[7])};
#pragma unroll
          for (int e = 0; e < 8; e++) {
            const float a = fms1(K[e], c1[e], c0[e]), b = fms1(K[e], s1[e], s0[e]);
            c0[e] = c1[e]; c1[e] = a; s0[e] = s1[e]; s1[e] = b; pin(c1[e]); pin(s1[e]);
          }
        }
        const unsigned m = (p & 1) ? 0x80000000u : 0u;
        *(LDSP u32x4*)(fa + (p % 3) * STG + i * 4096) = P[i][0] ^ m;
        *(LDSP u32x4*)(fa + (p % 3) * STG + i * 4096 + 16384) = P[i][1] ^ m;
        SB();
      };
      auto dxs = [&](int p) {
#pragma unroll
        for (int i = 0; i < 4; i++) dx(i, (p >> 1) + 4 * (p & 1), p % 3);
      };
      dxs(0); dxs(1); dxs(2);
#pragma unroll
      for (int e = 0; e < 8; e++) {
        const float f = (float)(f0 + e), a1 = f * (float)kk * (1.f / 512.f), st = f * (1.f / 32.f), a0 = a1 - st;
        c1[e] = cosr(a1); s1[e] = -sinr(a1); c0[e] = cosr(a0); s0[e] = -sinr(a0); K[e] = 2.f * cosr(st);
      }
      if (f0 == 0) s1[0] = s0[0] = (kk & 1) ? -1.f : 1.f;
      put(0, 0); put(0, 1); put(0, 2); put(0, 3);
      *(LDSP float*)(lds + L_CS + (64 * wi + lane) * 4) = (cb == 14 && wi == 0 && lane == 0) ? 512.f : 0.f;
      wait_v<8>(); WAIT_L0(); BAR();
      sa_load();
      put(1, 0); put(1, 1); put(1, 2); put(1, 3); put(2, 0); put(2, 1);
      wait_v<7>(); WAIT_L0(); BAR();
      dxs(3); wt_load();
      put(2, 2); put(2, 3); put(3, 0); put(3, 1);
      wait_v<22>(); WAIT_L0(); BAR();
      dxs(4);
      put(3, 2); put(3, 3); row_job(0); row_job(1); row_job(2); row_job(3); put(4, 0);
      wait_v<5>(); WAIT_L0(); BAR();
      dxs(5);
      put(4, 1); put(4, 2); put(4, 3); wt_pack();
      wait_v<5>(); WAIT_L0(); BAR();
      dxs(6); wt_store();
      put(5, 0); put(5, 1); put(5, 2); put(5, 3); put(6, 0); put(6, 1);
      wait_v<6>(); WAIT_L0(); BAR();
      dxs(7);
      put(6, 2); put(6, 3); put(7, 0); put(7, 1); put(7, 2); put(7, 3);
      wait_v<4>(); WAIT_L0(); BAR();
      gr = ald64s(rS, grv, 0);
#pragma unroll
      for (int j = 0; j < 4; j++) gc[j] = u32x2{0u, tag};
    }
    STAMP(1);
    st32s(tag, rS, (wi == 0 && lane == 0) ? SY_FW + bid * 4 : OOB);
    const rsrc_t rD = mk_rsrc(pwd, W1_BYTES);
    const unsigned dv = (lane >> 3) * 1024 + (((lane & 7) ^ (q << 1)) << 4);
    const unsigned k1 = 16 * wi + c;
    u32x4 bre[4], bim[4];
    float twc[2][4], tws[2][4], co[4], si[4];
    auto cjob = [&](int n) {
      if (n < 16) {
        const float a = (float)(k1 * (16 * (n >> 2) + 4 * q + (n & 3))) * (1.f / 64.f), x = cosr(a), y = sinr(a);
        unsigned u = pk2(x, y), v = pk2(-y, x); pin(u); pin(v);
        bre[n >> 2][n & 3] = u; bim[n >> 2][n & 3] = v;
      } else if (n < 24) {
        const float a = (float)(k1 * (16 * ((n >> 2) & 1) + 4 * q + (n & 3))) * (1.f / 2048.f);
        float x = cosr(a), y = sinr(a); pin(x); pin(y);
        twc[(n >> 2) & 1][n & 3] = x; tws[(n >> 2) & 1][n & 3] = y;
      } else if (n < 28) {
        const float a = (float)(c * (4 * q + (n & 3))) * (1.f / 32.f);
        float x = cosr(a), y = sinr(a); pin(x); pin(y);
        co[n & 3] = x; si[n & 3] = y;
      }
    };
    auto dd = [&](int buf, bool mine, int j, int n0) {
      const int S = mine ? 14 * wi + 5 + (j + nb) % 9 : 14 * wi + (j + nb) % 5;
#pragma unroll
      for (int p = 0; p < 4; p++) { dma16(rD, lds + bufaddr(buf, wi) + p * 1024, dv, (32 * S + 8 * p) * 1024 + nb * 128); cjob(n0 + p); SB(); }
    };
    dd(0, true, 0, 0); dd(1, true, 1, 4); dd(2, true, 2, 99);
    wait_v<12>();
    pin(gr); pin(gc[0]); pin(gc[1]); pin(gc[2]); pin(gc[3]);
#pragma unroll 1
    for (int it = 0; it < SPIN; it++) {
      if (__all(gr[1] == tag && gc[0][1] == tag && gc[1][1] == tag && gc[2][1] == tag && gc[3][1] == tag)) break;
      __builtin_amdgcn_s_sleep(1);
      asm volatile("buffer_load_dwordx2 %0, %1, %2, 0 offen sc1\n\ts_waitcnt vmcnt(0)" : "=v"(gr) : "v"(grv), "s"(rS) : "memory");
      if (!dft) {
#pragma unroll
        for (int j = 0; j < 4; j++) asm volatile("buffer_load_dwordx2 %0, %1, %2, %3 offen sc1\n\ts_waitcnt vmcnt(0)" : "=v"(gc[j]) : "v"(gcv), "s"(rS), "s"(j * 2048) : "memory");
      }
    }
    {
      const float sd = bflo(gr[0]), rr = __builtin_amdgcn_rcpf(sd);
      *(LDSP f32x4*)(lds + L_ST + (32 * wi + (lane & 31)) * 16) =
          f32x4{bfhi(gr[0]), dft ? rr : -2.302208198f * rr, -2.302208198f * 0.044715f * rr * rr * rr, sd * sd};
      if (!dft) *(LDSP float*)(lds + L_CS + (64 * wi + lane) * 4) = (asf(gc[0][0]) + asf(gc[1][0])) + (asf(gc[2][0]) + asf(gc[3][0]));
    }
    WAIT_L0(); BAR();
    dd(3, true, 3, 8); dd(4, true, 4, 12); dd(5, false, 0, 99);
    BAR();
    STAMP(2);
    dd(6, false, 1, 16); dd(7, false, 2, 20); dd(8, false, 3, 24);
    u32x4 Bq[5][4];
    wait_v<24>();
#pragma unroll
    for (int j = 0; j < 3; j++)
#pragma unroll
      for (int v = 0; v < 4; v++) { Bq[j][v] = ldstr2<2048>((tb ^ (v << 5)) + bufaddr(j, 0)); }
    WAIT_L0();
#pragma unroll
    for (int j = 0; j < 3; j++)
#pragma unroll
      for (int v = 0; v < 4; v++) pin(Bq[j][v]);
    dd(0, false, 4, 99); dd(1, true, 5, 99); dd(2, true, 6, 99);
    wait_v<24>();
#pragma unroll
    for (int j = 3; j < 5; j++)
#pragma unroll
      for (int v = 0; v < 4; v++) { Bq[j][v] = ldstr2<2048>((tb ^ (v << 5)) + bufaddr(j, 0)); }
    WAIT_L0();
#pragma unroll
    for (int j = 3; j < 5; j++)
#pragma unroll
      for (int v = 0; v < 4; v++) pin(Bq[j][v]);
    dd(3, true, 7, 99); dd(4, true, 8, 99);
    STAMP(3);
    wait_v<0>();
    *(volatile LDSP unsigned*)(lds + L_CN + CN_B + wi * 4) = 1;
    STAMP(4);
    const int f = (bid + 1) & 255;
#pragma unroll 1
    for (int it = 0; it < SPIN; it++) {
      const u32x4 a = ld128s(rS, SY_FW + lane * 16, 0);
      if (__all(a[0] == tag && a[1] == tag && a[2] == tag && a[3] == tag)) break;
      __builtin_amdgcn_s_sleep(1);
      asm volatile("" ::: "memory");
    }
    asm volatile("" ::: "memory");
    u32x4 wv[2], wx[3][2];
#pragma unroll
    for (int t = 0; t < 2; t++) wv[t] = ld128s(rS, OFF_WT + lane * 16 + t * 1024, f * 8192 + wi * 2048);
    poll(rS, SY_FT + (f >> 7) * 128 + c * 4, true, tag);
    STAMP(5);
    u32x4 xa[2][4];
#pragma unroll
    for (int j = 0; j < 4; j++)
#pragma unroll
      for (int ta = 0; ta < 2; ta++) xa[ta][j] = ld128s(rS, OFF_T + q * 512 + c * 16 + j * 2048 + ta * 256, f * 8192);
    SB();
    u32x4 c32[2], s32[2], n32[2];
    {
      const unsigned sg = (c & 1) ? 0x80008000u : 0u;
      const unsigned c01 = pk2(co[0], co[1]), c23 = pk2(co[2], co[3]), s01 = pk2(si[0], si[1]), s23 = pk2(si[2], si[3]);
      c32[0] = u32x4{c01, c23, c01 ^ sg, c23 ^ sg};
      s32[0] = u32x4{s01, s23, s01 ^ sg, s23 ^ sg};
      c32[1] = c32[0] ^ 0x80000000u; s32[1] = s32[0] ^ 0x80000000u;
      n32[0] = s32[0] ^ 0x80008000u; n32[1] = s32[1] ^ 0x80008000u;
    }
    f32x4 yr[2] = {z4, z4}, yi[2] = {z4, z4};
#pragma unroll
    for (int j = 0; j < 4; j++)
#pragma unroll
      for (int ta = 0; ta < 2; ta++) { yr[ta] = mfma(xa[ta][j], bre[j], yr[ta]); yi[ta] = mfma(xa[ta][j], bim[j], yi[ta]); }
    if (f == 0) {
#pragma unroll
      for (int n = 0; n < 3; n++)
#pragma unroll
        for (int t = 0; t < 2; t++) wx[n][t] = ld128s(rS, OFF_WT + lane * 16 + t * 1024, (256 + n) * 8192 + wi * 2048);
    }
    u32x4 pr, pi;
#pragma unroll
    for (int ta = 0; ta < 2; ta++) {
      float a[4], b[4];
#pragma unroll
      for (int e = 0; e < 4; e++) { a[e] = yr[ta][e] * twc[ta][e] + yi[ta][e] * tws[ta][e]; b[e] = yi[ta][e] * twc[ta][e] - yr[ta][e] * tws[ta][e]; }
      pr[2 * ta] = pk2(a[0], a[1]); pr[2 * ta + 1] = pk2(a[2], a[3]); pi[2 * ta] = pk2(b[0], b[1]); pi[2 * ta + 1] = pk2(b[2], b[3]);
    }
    f32x4 xr2[2], xi2[2];
#pragma unroll
    for (int t = 0; t < 2; t++) {
      xr2[t] = mfma(c32[t], pr, z4); xr2[t] = mfma(s32[t], pi, xr2[t]);
      xi2[t] = mfma(c32[t], pi, z4); xi2[t] = mfma(n32[t], pr, xi2[t]);
    }
    float fr[2][4], fi[2][4];
    unsigned gs = 4;
    if (f != 0) {
      const float sc = 1.f / 524288.f;
#pragma unroll
      for (int t = 0; t < 2; t++)
#pragma unroll
        for (int e = 0; e < 4; e++) {
          const float a = bflo(wv[t][e]) * sc, b = bfhi(wv[t][e]) * sc;
          fr[t][e] = xr2[t][e] * a - xi2[t][e] * b; fi[t][e] = xr2[t][e] * b + xi2[t][e] * a;
        }
    } else {
#pragma unroll
      for (int t = 0; t < 2; t++)
#pragma unroll
        for (int e = 0; e < 4; e++) *(LDSP unsigned*)(lds + L_U + (k1 + 64 * (16 * t + 4 * q + e)) * 4) = pk2(xr2[t][e], xi2[t][e]);
      WAIT_L0(); lds_arrive(lds + L_CN + CN_G, lane); lds_wait(lds + L_CN + CN_G, 4);
      const float sc = 0.25f / 1048576.f;
#pragma unroll
      for (int t = 0; t < 2; t++)
#pragma unroll
        for (int e = 0; e < 4; e++) {
          const unsigned pn = *(LDSP unsigned*)(lds + L_U + ((2048 - k1 - 64 * (16 * t + 4 * q + e)) & 2047) * 4);
          const unsigned d0 = wv[t][e], d1 = wx[0][t][e], d0n = wx[1][t][e], d1n = wx[2][t][e];
          const float ar = bflo(d0) + bflo(d0n), ai = bfhi(d0) - bfhi(d0n), br = bflo(d1) + bflo(d1n), bi = bfhi(d1) - bfhi(d1n);
          const float war = (ar + br) * sc, wai = (ai + bi) * sc, wbr = (ar - br) * sc, wbi = (ai - bi) * sc;
          fr[t][e] = xr2[t][e] * war - xi2[t][e] * wai + bflo(pn) * wbr + bfhi(pn) * wbi;
          fi[t][e] = xr2[t][e] * wai + xi2[t][e] * war + bflo(pn) * wbi - bfhi(pn) * wbr;
        }
      WAIT_L0(); lds_arrive(lds + L_CN + CN_G, lane); lds_wait(lds + L_CN + CN_G, 8);
      gs = 12;
    }
#pragma unroll
    for (int t = 0; t < 2; t++) {
      pr[2 * t] = pk2(fr[t][0], fr[t][1]); pr[2 * t + 1] = pk2(fr[t][2], fr[t][3]);
      pi[2 * t] = pk2(fi[t][0], fi[t][1]); pi[2 * t + 1] = pk2(fi[t][2], fi[t][3]);
    }
#pragma unroll
    for (int ta = 0; ta < 2; ta++) {
      f32x4 ur = mfma(c32[ta], pr, z4); ur = mfma(n32[ta], pi, ur);
      f32x4 ui = mfma(c32[ta], pi, z4); ui = mfma(s32[ta], pr, ui);
#pragma unroll
      for (int e = 0; e < 4; e++) {
        const float a = ur[e] * twc[ta][e] - ui[e] * tws[ta][e], b = ui[e] * twc[ta][e] + ur[e] * tws[ta][e];
        *(LDSP unsigned*)(lds + L_U + (16 * ta + 4 * q + e) * UROW + k1 * 4) = pk2(a, -b);
      }
    }
    STAMP(6);
    WAIT_L0(); lds_arrive(lds + L_CN + CN_G, lane); lds_wait(lds + L_CN + CN_G, gs);
#pragma unroll
    for (int ta = 0; ta < 2; ta++) {
      f32x4 zr = z4, zi = z4;
#pragma unroll
      for (int j = 0; j < 4; j++) {
        const u32x4 u = lds128(lds + L_U + (16 * ta + c) * UROW + (16 * j + 4 * q) * 4);
        zr = mfma(bre[j], u, zr); zi = mfma(bim[j], u, zi);
      }
      st128<SC1>(u32x4{pk2(zr[0], zi[0]), pk2(zr[1], zi[1]), pk2(zr[2], zi[2]), pk2(zr[3], zi[3])}, rS, OFF_Z + f * 8192 + wi * 2048 + q * 512 + c * 16 + ta * 256);
    }
    STAMP(7);
    {
      const unsigned fv = SY_FH + rb2 * 128 + (lane & 31) * 4;
      const bool need = lane >= ((14 * wi + 5) >> 2) && lane <= ((14 * wi + 13) >> 2);
      unsigned v = ald32s(rS, fv, 0);
      wait_v<0>(); pin(v);
      const unsigned old = lds_arrive(lds + L_CN + CN_Z, lane);
      st32s(tag, rS, (lane == 0 && old == 3) ? SY_FZ + f * 4 : OOB);
      if (!__all(!need || v == tag)) poll(rS, fv, need, tag);
    }
    STAMP(8);
    f32x4 a2[4][4];
    {
      u32x4 A[9][4];
      auto la = [&](int j) {
        const int S = 14 * wi + 5 + (j + nb) % 9;
#pragma unroll
        for (int r = 0; r < 4; r++) A[j][r] = ld128s(rS, hv + r * 2048, S * 8192);
      };
#pragma unroll
      for (int j = 0; j < 5; j++) la(j);
      SB();
#pragma unroll
      for (int j = 0; j < 9; j++) {
        u32x4 B[4];
#pragma unroll
        for (int v = 0; v < 4; v++) B[v] = j < 5 ? Bq[j < 5 ? j : 0][v] : ldstr2<2048>((tb ^ (v << 5)) + bufaddr(j - 4, 0));
#pragma unroll
        for (int r = 0; r < 4; r++)
#pragma unroll
          for (int v = 0; v < 4; v++) a2[r][v] = mfma(B[v], A[j][r], j ? a2[r][v] : z4);
        if (j < 4) { SB(); la(j + 5); SB(); }
      }
    }
    STAMP(9);
    const unsigned ov = (128 * rb2 + 32 * wi + 16 * half + c) * 1024 + nb * 128 + q * 16;
    u32x4 res[2];
#pragma unroll
    for (int h = 0; h < 2; h++) res[h] = __builtin_amdgcn_raw_buffer_load_b128(rX, ov + h * 64, 0, 0);
    WAIT_L0();
#pragma unroll
    for (int r = 0; r < 4; r++)
#pragma unroll
      for (int v = 0; v < 4; v++) *(LDSP f32x4*)(lds + bufaddr(1 + r, wi) + v * 1024 + lane * 16) = a2[r][v];
    WAIT_L0(); lds_arrive(lds + L_CN + CN_G, lane); lds_wait(lds + L_CN + CN_G, gs + 4);
    const unsigned ra = lds + (q >> 1) * 1024 + ((q & 1) * 32 + c) * 16;
    const unsigned r1 = ra + (wi < 2 ? 2 * STG + 4096 + wi * 4096 : (wi - 2) * 4096), r5 = ra + (wi ? STG + (wi - 1) * 4096 : 8192);
    f32x4 o[4];
#pragma unroll
    for (int n = 0; n < 4; n++) {
      o[n] = f32x4{bflo(res[n >> 1][2 * (n & 1)]), bfhi(res[n >> 1][2 * (n & 1)]), bflo(res[n >> 1][2 * (n & 1) + 1]), bfhi(res[n >> 1][2 * (n & 1) + 1])};
#pragma unroll
      for (int p = 0; p < 4; p++) o[n] += ldsf4(r1 + p * 12288 + (n >> 1) * 2048 + (n & 1) * 256);
      pin(o[n]);
    }
    STAMP(10);
    BAR();
    STAMP(11);
    const rsrc_t rO = mk_rsrc(pout, X_BYTES);
#pragma unroll
    for (int n = 0; n < 4; n++) {
      const unsigned a = r5 + (n >> 1) * 2048 + (n & 1) * 256;
      o[n] += (ldsf4(a) + ldsf4(a + 12288)) + (ldsf4(a + 24576) + ldsf4(a + 36864));
    }
#pragma unroll
    for (int h = 0; h < 2; h++)
      st128<0>(u32x4{pk2(o[2 * h][0], o[2 * h][1]), pk2(o[2 * h][2], o[2 * h][3]), pk2(o[2 * h + 1][0], o[2 * h + 1][1]), pk2(o[2 * h + 1][2], o[2 * h + 1][3])}, rO, ov + h * 64);
    STAMP(12);
  }
  FLUSH();
}

extern "C" void kernel_launch(void* const* d_in, const int* in_sizes, int n_in, void* d_out, int out_size, void* d_ws, size_t ws_size,
                              hipStream_t stream) {
  (void)in_sizes; (void)n_in; (void)out_size; (void)ws_size;
  (void)hipFuncSetAttribute((const void*)fused, hipFuncAttributeMaxDynamicSharedMemorySize, LDS_BYTES);
  fused<<<256, 512, LDS_BYTES, stream>>>(d_in[0], d_in[1], d_in[2], d_in[4], d_in[5], d_in[6], (char*)d_ws, d_out);
}
```

```cpp
#include <hip/hip_runtime.h>
#include <hip/hip_bf16.h>
#pragma clang diagnostic ignored "-Wint-to-pointer-cast"

typedef __bf16 bf16x8 __attribute__((ext_vector_type(8)));
typedef __bf16 bf16x2 __attribute__((ext_vector_type(2)));
typedef short s16x4 __attribute__((ext_vector_type(4)));
typedef float f32x4 __attribute__((ext_vector_type(4)));
typedef float f32x2 __attribute__((ext_vector_type(2)));
typedef unsigned u32x4 __attribute__((ext_vector_type(4)));
typedef unsigned u32x2 __attribute__((ext_vector_type(2)));
#define LDSP __attribute__((address_space(3)))
#define DI __device__ __forceinline__
typedef __amdgpu_buffer_rsrc_t rsrc_t;

constexpr unsigned X_BYTES = 2048u * 512u * 2u;
constexpr unsigned W1_BYTES = 512u * 1792u * 2u;
constexpr unsigned WF_BYTES = 2048u * 257u * 2u;
constexpr unsigned HB = 56u * 4096u, OFF_T = 32u * HB, OFF_Z = OFF_T + 256u * 8192u, OFF_WT = OFF_Z + 256u * 8192u, OFF_SY = OFF_WT + 256u * 8192u;
constexpr unsigned SY_E = OFF_SY, SY_FT = OFF_SY + 4096, SY_FZ = OFF_SY + 8192, SY_RG = OFF_SY + 16384,
                   SY_CG = OFF_SY + 32768, SY_FH = SY_CG + 14u * 8192u, WS_BYTES = SY_FH + 64u * 2048u;
constexpr unsigned OOB = 0x40000000u;
constexpr int SC1 = 16;
#define SPINV (1 << 20)
constexpr int SPIN = SPINV;
#define WIN 3
#define PDLY 6
#define HDLY 24
#define ZDLY 48
constexpr unsigned STG = 49152, L_U = 3 * STG, L_ST = L_U + 8704, L_CS = L_ST + 2048, L_CN = L_CS + 1024, L_DG = L_CN + 512, LDS_BYTES = 163840;
constexpr unsigned CN_X = 0, CN_S = 4, CN_LM = 16, CN_ML = 32;
constexpr unsigned UROW = 272, FRUN = 8224;

DI rsrc_t mk_rsrc(const void* p, unsigned bytes) { return __builtin_amdgcn_make_buffer_rsrc((void*)p, 0, bytes, 0x00020000); }
DI unsigned pk2(float a, float b) { bf16x2 v = {(__bf16)a, (__bf16)b}; return __builtin_bit_cast(unsigned, v); }
DI float bflo(unsigned u) { return __builtin_bit_cast(float, u << 16); }
DI float bfhi(unsigned u) { return __builtin_bit_cast(float, u & 0xffff0000u); }
DI float rbf(float a) { return (float)(__bf16)a; }
DI float asf(unsigned u) { return __builtin_bit_cast(float, u); }
DI unsigned asu(float f) { return __builtin_bit_cast(unsigned, f); }
DI f32x4 mfma(u32x4 a, u32x4 b, f32x4 c) {
  return __builtin_amdgcn_mfma_f32_16x16x32_bf16(__builtin_bit_cast(bf16x8, a), __builtin_bit_cast(bf16x8, b), c, 0, 0, 0);
}
DI float cosr(float rev) { return __builtin_amdgcn_cosf(rev); }
DI float sinr(float rev) { return __builtin_amdgcn_sinf(rev); }
template <int N> DI void wait_v() { asm volatile("s_waitcnt vmcnt(%0)" :: "i"(N) : "memory"); }
#define WAIT_L0() asm volatile("s_waitcnt lgkmcnt(0)" ::: "memory")
#define BAR() do { asm volatile("" ::: "memory"); __builtin_amdgcn_s_barrier(); asm volatile("" ::: "memory"); } while (0)
#define SB() __builtin_amdgcn_sched_barrier(0)
template <class T> DI void pin(T& v) { asm volatile("" : "+v"(v)); }
DI void dma16(rsrc_t r, unsigned la, unsigned li, unsigned voff, unsigned soff) {
  asm volatile("s_add_i32 m0, %0, %4\n\ts_nop 0\n\tbuffer_load_dwordx4 %1, %2, %3 offen lds" :: "s"(la), "v"(voff), "s"(r), "s"(soff), "i"(li) : "memory", "scc");
}
DI u32x4 ald128s(rsrc_t r, unsigned v, unsigned s) { u32x4 o; asm volatile("buffer_load_dwordx4 %0, %1, %2, %3 offen sc1" : "=v"(o) : "v"(v), "s"(r), "s"(s) : "memory"); return o; }
DI u32x2 ald64s(rsrc_t r, unsigned v, unsigned s) { u32x2 o; asm volatile("buffer_load_dwordx2 %0, %1, %2, %3 offen sc1" : "=v"(o) : "v"(v), "s"(r), "s"(s) : "memory"); return o; }
DI unsigned ald32s(rsrc_t r, unsigned v, unsigned s) { unsigned o; asm volatile("buffer_load_dword %0, %1, %2, %3 offen sc1" : "=v"(o) : "v"(v), "s"(r), "s"(s) : "memory"); return o; }
DI u32x4 ld128s(rsrc_t r, unsigned v, unsigned s) { return __builtin_amdgcn_raw_buffer_load_b128(r, v, s, SC1); }
template <int AUX> DI void st128(u32x4 v, rsrc_t r, unsigned voff) { __builtin_amdgcn_raw_buffer_store_b128(v, r, voff, 0, AUX); }
DI void st64s(u32x2 v, rsrc_t r, unsigned voff) { __builtin_amdgcn_raw_buffer_store_b64(v, r, voff, 0, SC1); }
DI void st32s(unsigned v, rsrc_t r, unsigned voff) { __builtin_amdgcn_raw_buffer_store_b32(v, r, voff, 0, SC1); }
DI u32x4 lds128(unsigned a) { return *(LDSP u32x4*)a; }
DI f32x4 ldsf4(unsigned a) { return *(LDSP f32x4*)a; }
DI u32x2 ldstr(unsigned a) { return __builtin_bit_cast(u32x2, __builtin_amdgcn_ds_read_tr16_b64_v4i16((LDSP s16x4*)a)); }
template <int D> DI u32x4 ldstr2(unsigned a) { const u32x2 lo = ldstr(a), hi = ldstr(a + D); return u32x4{lo[0], lo[1], hi[0], hi[1]}; }
template <int C> DI float dpp_add(float v) { return v + asf(__builtin_amdgcn_update_dpp(0, asu(v), C, 0xf, 0xf, true)); }
DI float swap16_add(float a, float b) { const auto r = __builtin_amdgcn_permlane16_swap(asu(a), asu(b), false, false); return asf(r[0]) + asf(r[1]); }
DI float swap32_add(float a, float b) { const auto r = __builtin_amdgcn_permlane32_swap(asu(a), asu(b), false, false); return asf(r[0]) + asf(r[1]); }
DI float sum16(float v) { v = dpp_add<0xB1>(v); v = dpp_add<0x4E>(v); v = dpp_add<0x141>(v); return dpp_add<0x128>(v); }
DI float dot2(unsigned a, unsigned b, float s) { return __builtin_amdgcn_fdot2_f32_bf16(__builtin_bit_cast(bf16x2, a), __builtin_bit_cast(bf16x2, b), s, false); }
DI void swp(u32x4& a, u32x4& b) {
#pragma unroll
  for (int e = 0; e < 4; e++) { unsigned x = a[e], y = b[e]; asm volatile("v_swap_b32 %0, %1" : "+v"(x), "+v"(y)); a[e] = x; b[e] = y; }
}
DI float add1(float a, float b) { float o; asm("v_add_f32 %0, %1, %2" : "=v"(o) : "v"(a), "v"(b)); return o; }
template <int NAP> DI void poll(rsrc_t r, unsigned voff, unsigned tag) {
#pragma unroll 1
  for (int it = 0; it < SPIN; it++) {
    unsigned v = ald32s(r, voff, 0);
    wait_v<0>(); pin(v);
    if (__all(voff == OOB || v == tag)) break;
    __builtin_amdgcn_s_sleep(NAP);
  }
}
DI void lds_wait(unsigned a, unsigned n) {
#pragma unroll 1
  for (int it = 0; it < SPIN * 8; it++) { if (*(volatile LDSP unsigned*)a >= n) break; __builtin_amdgcn_s_sleep(1); }
  asm volatile("" ::: "memory");
}
DI void lds_arrive(unsigned a, unsigned lane) {
  if (lane == 0) __hip_atomic_fetch_add((LDSP unsigned*)a, 1u, __ATOMIC_RELAXED, __HIP_MEMORY_SCOPE_WORKGROUP);
  asm volatile("" ::: "memory");
}
DI void lds_set(unsigned a, unsigned v) { asm volatile("" ::: "memory"); *(volatile LDSP unsigned*)a = v; asm volatile("" ::: "memory"); }
DI void dma16c(int nb, rsrc_t r, unsigned la, unsigned li, unsigned voff, unsigned soff) {
  asm volatile("s_add_i32 m0, %0, %5\n\ts_cmp_eq_u32 %4, 7\n\ts_cbranch_scc1 1f\n\tbuffer_load_dwordx4 %1, %2, %3 offen lds\n1:"
               :: "s"(la), "v"(voff), "s"(r), "s"(soff), "s"(nb), "i"(li) : "memory", "scc");
}
template <int N> DI void wait_c(int nb) {
  asm volatile("s_waitcnt vmcnt(%0)\n\ts_cmp_eq_u32 %1, 7\n\ts_cbranch_scc0 1f\n\ts_waitcnt vmcnt(0)\n1:" :: "i"(N), "s"(nb) : "memory", "scc");
}
DI u32x4 ald128(rsrc_t r, unsigned v) { u32x4 o; asm volatile("buffer_load_dwordx4 %0, %1, %2, 0 offen" : "=v"(o) : "v"(v), "s"(r) : "memory"); return o; }
constexpr unsigned bufaddr(int b) { return (b < 3 ? 2 * STG : b < 6 ? 0 : STG) + (b % 3) * 4096; }
#define PRIO(n) __builtin_amdgcn_s_setprio(n)

#define STAMP(i)
#define CLK(i)
#define FLUSH()
#define STAMP2(i)

__global__ __launch_bounds__(512, 2) void fused(const void* __restrict__ px, const void* __restrict__ pwr, const void* __restrict__ pwi,
                                                const void* __restrict__ pwg, const void* __restrict__ pwu, const void* __restrict__ pwd,
                                                char* ws, void* __restrict__ pout) {
  extern __shared__ __attribute__((aligned(16))) char lds_[];
  const unsigned lds = (unsigned)(size_t)(LDSP char*)lds_;
  asm volatile("" :: "s"(px), "s"(pwr), "s"(pwi), "s"(pwg), "s"(pwu), "s"(ws));
  const int bid = blockIdx.x, xcd = bid & 7, idx = bid >> 3;
  const int rb = (xcd & 3) * 4 + (idx & 3), half = xcd >> 2, nb = idx >> 2, cb = (7 * half + nb) % 14;
  const int pid = 16 * rb + 8 * half + nb, slot = 32 * xcd + idx;
  const bool helper = nb == 7;
  const int wave = __builtin_amdgcn_readfirstlane(threadIdx.x >> 6), wi = wave & 3;
  const unsigned lane = threadIdx.x & 63, c = lane & 15, q = lane >> 4;
  const rsrc_t rS = mk_rsrc(ws, WS_BYTES), rX = mk_rsrc(px, X_BYTES);
  const f32x4 z4 = {0.f, 0.f, 0.f, 0.f};
  unsigned tag;
  STAMP(0);

  auto step = [&](int n) { const int t = cb + n; return 4 * (t >= 14 ? t - 14 : t) + wi; };
  auto tbase = [&]() { return lds + wi * 12288 + (4 * q + (c >> 2)) * 128 + (((2 * q + (c >> 3)) & 3) << 5) + (c & 3) * 8; };
  auto ldb = [&](u32x4 (&B)[4], unsigned tb, int buf) {
#pragma unroll
    for (int v = 0; v < 4; v++) B[v] = ldstr2<2048>((tb ^ (v << 5)) + bufaddr(buf));
  };
  auto hbase = [&](int p) { return (2 * rb + half) * HB + ((p ^ 3 ^ wi) << 10) + lane * 16; };
  auto obase = [&]() { return (128 * rb + 32 * wi + 16 * half + c) * 1024 + nb * 128 + (wave >> 2) * 64 + (q & 1) * 32 + (q >> 1) * 16; };
  const unsigned cLM = lds + L_CN + CN_LM + wi * 4, cML = lds + L_CN + CN_ML + wi * 4;
  f32x4 o0, o1;
  u32x4 res;

  if (wave < 4) {
    f32x4 acc[8][4];
    {
      const unsigned row0 = 128 * rb + 64 * (wi >> 1) + 16 * half + 2 * nb + (wi & 1), ra = lds + 2 * STG + wi * 2048;
      dma16(rX, ra, 0, lane * 16, row0 * 1024);
      dma16(rX, ra, 1024, lane * 16, row0 * 1024 + 32768);
      unsigned ev = ald32s(rS, SY_E, 0);
      if (wave == 0 && lane < 16) *(LDSP unsigned*)(lds + L_CN + lane * 4) = 0;
      u32x4 C[2], N[2], A2;
      f32x4 tc[2], ts[2];
      {
        float co[4], si[4], ca[4], sa[4];
#pragma unroll
        for (int e = 0; e < 4; e++) {
          const float n1 = (float)(4 * q + e), a = n1 * (float)(c + 1);
          co[e] = cosr(a * (1.f / 32.f)); si[e] = -sinr(a * (1.f / 32.f));
          tc[0][e] = cosr(a * (1.f / 512.f)); ts[0][e] = sinr(a * (1.f / 512.f));
          tc[1][e] = cosr(a * (1.f / 512.f) + n1 * (1.f / 32.f)); ts[1][e] = sinr(a * (1.f / 512.f) + n1 * (1.f / 32.f));
          const float b = n1 * (float)(c >> 1) * (1.f / 16.f) + (float)(c & 1) * 0.25f;
          ca[e] = cosr(b); sa[e] = sinr(b);
        }
        const unsigned sg = (c & 1) ? 0u : 0x80008000u;
        const unsigned c01 = pk2(co[0], co[1]), c23 = pk2(co[2], co[3]), s01 = pk2(si[0], si[1]), s23 = pk2(si[2], si[3]);
        C[0] = u32x4{c01, c23, c01 ^ sg, c23 ^ sg}; N[0] = u32x4{s01, s23, s01 ^ sg, s23 ^ sg};
        C[1] = C[0] ^ 0x80000000u; N[1] = N[0] ^ 0x80000000u;
        A2 = u32x4{pk2(ca[0], ca[1]), pk2(ca[2], ca[3]), pk2(sa[0], sa[1]), pk2(sa[2], sa[3])};
        pin(C[0]); pin(C[1]); pin(N[0]); pin(N[1]); pin(A2); pin(tc[0]); pin(tc[1]); pin(ts[0]); pin(ts[1]);
      }
#pragma unroll
      for (int t = 0; t < 8; t++)
#pragma unroll
        for (int m = 0; m < 4; m++) { acc[t][m] = z4; pin(acc[t][m]); }
      wait_v<0>();
      pin(ev);
      tag = __builtin_amdgcn_readfirstlane(ev) + 1;
      STAMP(13);
      u32x4 X[2];
      float s[2], p[2];
#pragma unroll
      for (int n = 0; n < 2; n++) X[n] = ldstr2<512>(ra + n * 1024 + lane * 8);
#pragma unroll
      for (int n = 0; n < 2; n++) {
        s[n] = p[n] = 0.f;
#pragma unroll
        for (int e = 0; e < 4; e++) { s[n] = dot2(X[n][e], 0x3f803f80u, s[n]); p[n] = dot2(X[n][e], X[n][e], p[n]); }
      }
      const float sp = sum16(swap16_add(swap32_add(s[0], s[1]), swap32_add(p[0], p[1])));
      const auto w = __builtin_amdgcn_permlane16_swap(asu(sp), asu(sp), false, false);
      const float m = asf(w[0]) * (1.f / 512.f), var = fmaxf(asf(w[1]) * (1.f / 512.f) - m * m, 0.f);
      const unsigned st = pk2(__builtin_amdgcn_sqrtf(rbf(rbf(var) + 1e-5f)), m);
      st64s(u32x2{st, tag}, rS, (lane & 31) ? OOB : SY_RG + (row0 + lane) * 8);
      const unsigned mb = st & 0xffff0000u, rr = asu(__builtin_amdgcn_rcpf(bflo(st)));
      const float mk = c == 15 ? -32.f : 0.f;
      u32x2 o[4];
#pragma unroll
      for (int n = 0; n < 2; n++) {
        const float mu = asf(__builtin_amdgcn_readlane(mb, 32 * n)), rs = asf(__builtin_amdgcn_readlane(rr, 32 * n));
#pragma unroll
        for (int h = 0; h < 2; h++) {
          f32x4 yr = mfma(X[n], C[h], z4);
          const f32x4 yi = mfma(X[n], N[h], z4);
          if (h) yr += mk * mu;
          const f32x4 a = yr * tc[h] + yi * ts[h], b = yi * tc[h] - yr * ts[h];
          const f32x4 t = mfma(A2, u32x4{pk2(a[0], a[1]), pk2(a[2], a[3]), pk2(b[0], b[1]), pk2(b[2], b[3])}, z4) * rs;
          o[h][n] = pk2(t[0], t[1]); o[2 + h][n] = pk2(t[2], t[3]);
        }
      }
      unsigned ta = lds + L_U + (64 * q + c) * 32 + (wi & 1) * 16 + (wi >> 1) * 8;
      pin(ta);
#pragma unroll
      for (int u = 0; u < 4; u++) *(LDSP u32x2*)(ta + 512 * u) = o[u];
      STAMP(14);
    }
    u32x4 A[9][4];
    if (!helper) {
      PRIO(2);
      unsigned xa0 = lds + c * 128 + ((q ^ (c >> 1)) << 4), xa1 = xa0 ^ 64;
      const unsigned key = (c >> 2) | ((q & 1) << 2);
      unsigned wa0 = lds + 16384 + (8 * q + (c >> 2)) * 256 + (((2 * wi) ^ key) << 5) + (c & 3) * 8, wa1 = wa0 ^ 32;
      u32x4 W[2][4], X[2][4];
      auto lw = [&](int b, int m0, int ks) {
#pragma unroll
        for (int m = m0; m < m0 + 2; m++) W[b][m] = ldstr2<1024>(((m & 1) ? wa1 : wa0) + ks * 8192 + (m >> 1) * 16384);
      };
      auto lx = [&](int h, int ks) {
#pragma unroll
        for (int t = 0; t < 4; t++) X[h][t] = lds128((ks ? xa1 : xa0) + (h * 4 + t) * 2048);
      };
      auto mm = [&](int b, int h, int t0, int t1) {
#pragma unroll
        for (int t = t0; t < t1; t++)
#pragma unroll
          for (int m = 0; m < 4; m++) acc[h * 4 + t][m] = mfma(W[b][m], X[h][t], acc[h * 4 + t][m]);
#pragma unroll
        for (int i = 0; i < (t1 - t0) * 4; i++) { __builtin_amdgcn_sched_group_barrier(0x008, 1, 0); __builtin_amdgcn_sched_group_barrier(0x102, 1, 0); }
        SB();
      };
      WAIT_L0(); BAR();
      STAMP(1); CLK(29);
      lw(0, 0, 0); lw(0, 2, 0); lx(0, 0);
      int sl = 0;
#pragma unroll 1
      for (int s = 0; s < 8; s++) {
        const unsigned dl = (sl == 2) ? -2 * STG : STG;
        sl = (sl == 2) ? 0 : sl + 1;
        lx(1, 0); lw(1, 0, 1); mm(0, 0, 0, 4);
        lw(1, 2, 1); lx(0, 1); mm(0, 1, 0, 4);
        lx(1, 1); xa0 += dl; xa1 += dl; wa0 += dl; wa1 += dl; mm(1, 0, 0, 3);
        pin(xa0); pin(xa1); pin(wa0); pin(wa1);
        STAMP2(15 + s);
        asm volatile("s_waitcnt lgkmcnt(0)\n\ts_cmp_eq_u32 %0, 7\n\ts_cbranch_scc1 1f\n\ts_barrier\n1:" :: "s"(s) : "memory", "scc");
        lw(0, 0, 0); mm(1, 0, 3, 4);
        lw(0, 2, 0); lx(0, 0); mm(1, 1, 0, 4);
      }
      lds_arrive(lds + L_CN + CN_S, lane);
      STAMP(2); CLK(30);
      f32x4 cs[4], st[8];
      unsigned ac = lds + L_CS + 128 * wi + 16 * q, as = lds + L_ST + c * 16;
      pin(ac); pin(as);
      st[0] = ldsf4(as);
#pragma unroll
      for (int m = 0; m < 4; m++) cs[m] = ldsf4(ac + (m >> 1) * 512 + 64 * (m & 1));
#pragma unroll
      for (int t = 1; t < 8; t++) st[t] = ldsf4(as + 256 * t);
      SB();
      const unsigned ho = (cb * 4 + wi) * 4096 + lane * 16;
      const unsigned ho0 = ho + (2 * rb + half) * HB, ho1 = ho + (2 * rb + 1 - half) * HB;
#pragma unroll
      for (int t = 0; t < 8; t++) {
        f32x2 g[4], u[4], y[4];
#pragma unroll
        for (int p = 0; p < 4; p++) { const int v = p >> 1, e = (p & 1) * 2; g[p] = f32x2{acc[t][v][e], acc[t][v][e + 1]} - st[t][0] * f32x2{cs[v][e], cs[v][e + 1]}; }
#pragma unroll
        for (int p = 0; p < 4; p++) y[p] = g[p] * g[p];
        SB();
#pragma unroll
        for (int p = 0; p < 4; p++) y[p] = st[t][1] + st[t][2] * y[p];
#pragma unroll
        for (int p = 0; p < 4; p++) y[p] *= g[p];
        SB();
#pragma unroll
        for (int p = 0; p < 4; p++) {
          const int v = p >> 1, e = (p & 1) * 2;
          y[p] = f32x2{__builtin_amdgcn_exp2f(y[p][0]), __builtin_amdgcn_exp2f(y[p][1])};
          u[p] = f32x2{acc[t][2 + v][e], acc[t][2 + v][e + 1]} - st[t][0] * f32x2{cs[2 + v][e], cs[2 + v][e + 1]};
        }
        SB();
#pragma unroll
        for (int p = 0; p < 4; p++) y[p] = st[t][3] + st[t][3] * y[p];
        SB();
#pragma unroll
        for (int p = 0; p < 4; p++) { y[p] = f32x2{__builtin_amdgcn_rcpf(y[p][0]), __builtin_amdgcn_rcpf(y[p][1])}; u[p] *= g[p]; }
        SB();
#pragma unroll
        for (int p = 0; p < 4; p++) u[p] *= y[p];
        SB();
        const u32x4 o = {pk2(u[0][0], u[0][1]), pk2(u[1][0], u[1][1]), pk2(u[2][0], u[2][1]), pk2(u[3][0], u[3][1])};
        st128<SC1>(o, rS, ((t & 1) ? ho1 : ho0) + (t >> 1) * 1024);
        if (!(t & 1)) A[0][t >> 1] = o;
      }
      SB();
    } else {
      WAIT_L0();
#pragma unroll 1
      for (int s = 0; s < 8; s++) BAR();
      lds_arrive(lds + L_CN + CN_S, lane);
#pragma unroll
      for (int p = 0; p < 4; p++) A[0][p] = u32x4{0, 0, 0, 0};
    }
    STAMP(3);
    lds_wait(cLM, 1);
    STAMP(4);
    f32x4 a2[4][4];
    {
      u32x4 G[4][4], Z[16];
      const unsigned zv = OFF_Z + 16 * q * 8192 + rb * 512 + (16 * half + c) * 16;
#pragma unroll
      for (int v = 0; v < 4; v++) {
        Z[4 * v] = ald128s(rS, zv, (64 * wi + 4 * v) * 8192);
        SB();
        const float d = (float)(64 * nb + 16 * v + c) * (1.f / 512.f), a = d * (float)(64 * wi + 16 * q);
        f32x2 u0 = {cosr(a), sinr(a)}, u1 = {cosr(a + d), sinr(a + d)};
        const float k = 2.f * cosr(d);
#pragma unroll
        for (int j = 0; j < 16; j++) {
          G[j >> 2][v][j & 3] = pk2(u1[0], u1[1]);
          const f32x2 u2 = k * u1 - u0; u0 = u1; u1 = u2;
          if ((j & 3) == 3) { pin(G[j >> 2][v]); if (j < 12) Z[4 * v + 1 + (j >> 2)] = ald128s(rS, zv, (64 * wi + 4 * v + 1 + (j >> 2)) * 8192); SB(); }
        }
        if (v == 2) {
          wait_v<12>();
          st32s(tag, rS, (lane == 0 && !helper) ? SY_FH + (rb * 4 + wi) * 2048 + cb * 128 : OOB);
          lds_set(cML, 1);
          STAMP(5);
        }
      }
      res = ald128(rX, obase());
      if (!(wi & 1)) { swp(A[0][0], A[0][1]); swp(A[0][2], A[0][3]); }
      if (!(wi & 2)) { swp(A[0][0], A[0][2]); swp(A[0][1], A[0][3]); }
      STAMP(6);
#pragma unroll
      for (int zz = 0; zz < 4; zz++) {
        if (zz == 0) wait_v<6>();
        if (zz == 3) wait_v<1>();
#pragma unroll
        for (int j = 0; j < 4; j++) pin(Z[4 * zz + j]);
#pragma unroll
        for (int p = 0; p < 4; p++) {
          const u32x4 zf = {Z[4 * zz][p], Z[4 * zz + 1][p], Z[4 * zz + 2][p], Z[4 * zz + 3][p]};
#pragma unroll
          for (int v = 0; v < 4; v++) a2[p][v] = mfma(G[zz][v], zf, zz ? a2[p][v] : z4);
        }
        SB();
      }
    }
    STAMP(7);
    {
      unsigned hv[4];
#pragma unroll
      for (int p = 0; p < 4; p++) { hv[p] = hbase(p); pin(hv[p]); }
      const unsigned tb = tbase();
      constexpr int bf[9] = {5, 0, 1, 2, 3, 4, 6, 7, 8};
      u32x4 B[2][4];
      lds_wait(cLM, 2);
      STAMP(8);
      ldb(B[0], tb, bf[0]);
      if (helper) {
#pragma unroll
        for (int p = 0; p < 4; p++) asm volatile("buffer_load_dwordx4 %0, %1, %2, %3 offen sc1" : "+v"(A[0][p]) : "v"(hv[p]), "s"(rS), "s"(step(0) * 4096) : "memory");
      }
#pragma unroll
      for (int j = 1; j <= WIN; j++)
#pragma unroll
        for (int p = 0; p < 4; p++) A[j][p] = ald128s(rS, hv[p], step(j) * 4096);
      SB();
#pragma unroll
      for (int j = 0; j < 9; j++) {
        if (j < 8) ldb(B[(j + 1) & 1], tb, bf[j + 1]);
        if (j == 2) lds_set(cML, 2);
        SB();
#pragma unroll
        for (int p = 0; p < 4; p++) {
          if (j + WIN < 8) wait_v<4 * WIN + 3>(); else {
            const int n = 3 - p + 4 * (8 - j);
            asm volatile("s_waitcnt vmcnt(%0)" :: "i"(n) : "memory");
          }
          pin(A[j][p]);
#pragma unroll
          for (int v = 0; v < 4; v++) a2[p][v] = mfma(B[j & 1][v], A[j][p], a2[p][v]);
          SB();
          if (j + WIN < 8) A[j + WIN + 1][p] = ald128s(rS, hv[p], step(j + WIN + 1) * 4096);
          if (j == 8 && p) {
#pragma unroll
            for (int v = 0; v < 4; v++) *(LDSP f32x4*)(lds + wi * 12288 + bufaddr(p == 3 ? 6 : 2 + p) + v * 1024 + lane * 16) = a2[p - 1][v];
          }
          SB();
        }
      }
#pragma unroll
      for (int v = 2; v < 4; v++) *(LDSP f32x4*)(lds + wi * 12288 + bufaddr(2) + v * 1024 + lane * 16) = a2[3][v];
      o0 = a2[3][0]; o1 = a2[3][1];
    }
    STAMP(9);
  } else {
    const rsrc_t rG = mk_rsrc(pwg, W1_BYTES), rU = mk_rsrc(pwu, W1_BYTES);
    const unsigned xv0 = (lane >> 3) * 1024 + (((lane & 7) ^ q) << 4), xv1 = xv0 ^ 64;
    const unsigned wv0 = q * 3584 + ((c ^ (q << 1)) << 4), wv1 = wv0 ^ 128;
    auto stage = [&](int s, auto&& job) {
      const int ks = (s + 2 * (rb >> 2)) & 7, sl = s % 3;
#pragma unroll
      for (int i = 0; i < 12; i++) {
        if (i < 4) dma16c(nb, rX, lds + wi * 4096, sl * STG + i * 1024, (i & 1) ? xv1 : xv0, (rb * 128 + ((4 * wi + i) ^ (2 * half)) * 8) * 1024 + ks * 128);
        else dma16c(nb, (i & 1) ? rU : rG, lds + wi * 4096, sl * STG + 16384 + (i & 1) * 16384 + ((i - 4) >> 1) * 1024, (i & 8) ? wv1 : wv0,
                    (64 * ks + 16 * wi + 4 * ((i - 4) >> 1)) * 3584 + cb * 256);
        job(i); SB();
      }
    };
    const int hid = 2 * rb + half;
    if (helper) {
      const unsigned fo = (16 * (hid >> 3) + 256 * (hid & 7) + 64 * wi) * 514;
#pragma unroll
      for (int a = 0; a < 2; a++) {
        const rsrc_t rF = mk_rsrc(a ? pwi : pwr, WF_BYTES);
#pragma unroll
        for (int n = 0; n < 8; n++) dma16(rF, lds + 2 * wi * FRUN, a * FRUN + n * 1024, lane * 16, fo + n * 1024);
        if (lane < 2) dma16(rF, lds + 2 * wi * FRUN, a * FRUN + 8192, lane * 16, fo + 8192);
      }
    }
    stage(0, [](int) {});
    stage(1, [](int) {});
    unsigned ev = ald32s(rS, SY_E, 0);
    const unsigned cp = c ^ 3 ^ (slot >> 6), k1 = 16 * wi + cp;
    u32x4 bre[4], bim[4], c32[2], s32[2], n32[2];
    float twc[2][4], tws[2][4], co[4], si[4];
    auto cjob = [&](int n) {
      if (n < 4) {
        const float a = (float)(k1 * (4 * q + n)) * (1.f / 64.f), x = cosr(a), y = sinr(a);
        unsigned u = pk2(x, y), v = pk2(-y, x); pin(u); pin(v);
        bre[0][n] = u; bim[0][n] = v;
      } else if (n < 8) {
        const unsigned u = bre[0][n & 3], v = bim[0][n & 3], m2 = (k1 & 1) ? 0x80008000u : 0u, m1 = (k1 & 2) ? 0x80008000u : 0u;
        unsigned u1 = ((k1 & 1) ? v : u) ^ m1, v1 = ((k1 & 1) ? u : v) ^ m1 ^ m2, u2 = u ^ m2, v2 = v ^ m2, u3 = u1 ^ m2, v3 = v1 ^ m2;
        pin(u1); pin(v1); pin(u2); pin(v2); pin(u3); pin(v3);
        bre[1][n & 3] = u1; bim[1][n & 3] = v1; bre[2][n & 3] = u2; bim[2][n & 3] = v2; bre[3][n & 3] = u3; bim[3][n & 3] = v3;
      } else if (n < 16) {
        const float a = (float)(k1 * (16 * ((n >> 2) & 1) + 4 * q + (n & 3))) * (1.f / 2048.f);
        float x = cosr(a), y = sinr(a); pin(x); pin(y);
        twc[(n >> 2) & 1][n & 3] = x; tws[(n >> 2) & 1][n & 3] = y;
      } else if (n < 20) {
        const float a = (float)(c * (4 * q + (n & 3))) * (1.f / 32.f);
        float x = cosr(a), y = sinr(a); pin(x); pin(y);
        co[n & 3] = x; si[n & 3] = y;
      } else if (n == 20) {
        const unsigned sg = (c & 1) ? 0x80008000u : 0u;
        const unsigned c01 = pk2(co[0], co[1]), c23 = pk2(co[2], co[3]), s01 = pk2(si[0], si[1]), s23 = pk2(si[2], si[3]);
        c32[0] = u32x4{c01, c23, c01 ^ sg, c23 ^ sg}; s32[0] = u32x4{s01, s23, s01 ^ sg, s23 ^ sg};
        pin(c32[0]); pin(s32[0]);
      } else if (n == 21) {
        c32[1] = c32[0] ^ 0x80000000u; s32[1] = s32[0] ^ 0x80000000u; pin(c32[1]); pin(s32[1]);
      } else if (n == 22) {
        n32[0] = s32[0] ^ 0x80008000u; n32[1] = s32[1] ^ 0x80008000u; pin(n32[0]); pin(n32[1]);
      }
    };
    const unsigned ca = lds + 16384 + (rb & 2) * 8192 + (((c >> 1) & 3) + 8 * (c >> 3) + 4 * (q & 1) + 16 * (q >> 1)) * 256 +
                        (((4 * (rb & 1) + wi) ^ (c >> 1)) << 5) + (c & 1) * 16;
    u32x4 sel, cr[2];
    f32x4 cs = z4;
    auto col_job = [&](int s, int i) {
      if (i == 1) { cr[0] = lds128(ca + s * STG); cr[1] = lds128(ca + s * STG + 8192); }
      if (i == 4) { PRIO(3); cs = mfma(cr[0], sel, cs); cs = mfma(cr[1], sel, cs); pin(cs); SB(); PRIO(0); }
    };
#pragma unroll
    for (int e = 0; e < 4; e++) sel[e] = ((c >> 1) & 3) == e ? ((c & 1) ? 0x3f800000u : 0x3f80u) : 0u;
    pin(sel);
    unsigned lu = lds + L_U + lane * 16, luw = lu + wi * 2048, uw = lds + L_U + 4 * q * UROW + k1 * 4, ur = lds + L_U + c * UROW + 16 * q;
    pin(lu); pin(luw); pin(uw); pin(ur);
    wait_c<13>(nb); BAR();
    {
      const u32x4 t0 = lds128(luw), t1 = lds128(luw + 1024);
      SB();
      stage(2, [&](int i) {
        if (i == 0) { const unsigned a = OFF_T + pid * 8192 + wi * 2048 + lane * 16; st128<SC1>(t0, rS, a); st128<SC1>(t1, rS, a + 1024); }
        col_job(0, i);
      });
    }
    if (helper) {
      const unsigned od = c & 1, sal = od ? 0x07060504u : 0x05040302u, slo = od ? 0x01000504u : 0x05040100u, shi = slo + 0x02020202u;
#pragma unroll 1
      for (int it = 0; it < 2; it++) {
        const unsigned g = 4 * q + wi + 16 * it;
        const unsigned a0 = lds + c * 514 - 2 * od + 16 * g, aa = a0 + od * FRUN, ab = a0 + FRUN - od * FRUN;
        u32x4 w[8];
#pragma unroll
        for (int e = 0; e < 4; e++) {
          unsigned x[5], y[5];
#pragma unroll
          for (int j = 0; j < 5; j++) { x[j] = *(LDSP unsigned*)(aa + e * 2 * FRUN + j * 4); y[j] = *(LDSP unsigned*)(ab + e * 2 * FRUN + j * 4); }
#pragma unroll
          for (int j = 0; j < 4; j++) {
            const unsigned u = __builtin_amdgcn_perm(x[j + 1], x[j], sal), v = __builtin_amdgcn_perm(y[j + 1], y[j], sal);
            w[2 * j][e] = __builtin_amdgcn_perm(v, u, slo); w[2 * j + 1][e] = __builtin_amdgcn_perm(v, u, shi);
          }
        }
#pragma unroll
        for (int j = 0; j < 8; j++) st128<SC1>(w[j], rS, OFF_WT + (8 * g + j) * 8192 + hid * 256 + c * 16);
      }
    }
    STAMP2(13); wait_c<14>(nb); WAIT_L0(); BAR(); STAMP2(14);
    pin(ev);
    tag = __builtin_amdgcn_readfirstlane(ev) + 1;
    stage(3, [&](int i) {
      if (i == 3) { wait_v<15>(); st32s(tag, rS, lane == 0 ? SY_FT + wi * 1024 + pid * 4 : OOB); }
      col_job(1, i);
      if (i >= 5) cjob(i - 5);
    });
    STAMP2(15); wait_v<13>(); BAR(); STAMP2(16);
    stage(4, [&](int i) {
      if (i == 0) {
        float v = swap32_add(cs[0] + cs[2], cs[1] + cs[3]);
        v = swap16_add(v, v);
        st64s(u32x2{asu(v), tag}, rS, ((lane & 24) || helper) ? OOB : SY_CG + cb * 8192 + (64 * (rb & 3) + 16 * wi + 8 * (lane >> 5) + c) * 32 + (rb >> 2) * 8);
      } else cjob(6 + i);
    });
    STAMP2(17); wait_v<13>(); BAR(); STAMP2(18);
    u32x4 xa[2][4], wv[2], pr, pi;
    {
      const unsigned fv = SY_FT + (slot >> 6) * 1024 + (64 * wi + lane) * 4;
      unsigned ft = ald32s(rS, fv, 0);
      stage(5, [&](int i) {
        if (i < 5) cjob(18 + i);
        if (i == 5) {
          wait_c<6>(nb); pin(ft);
          if (!__all(ft == tag)) poll<4>(rS, fv, tag);
          STAMP(1);
#pragma unroll
          for (int ta = 0; ta < 2; ta++) xa[ta][0] = ald128s(rS, OFF_T + (16 * q + (c >> 1)) * 8192 + (c & 1) * 16, (64 * wi + 8 * ta) * 8192 + slot * 32);
#pragma unroll
          for (int t = 0; t < 2; t++) wv[t] = ald128s(rS, OFF_WT + q * 256 + cp * 16, slot * 8192 + wi * 2048 + t * 1024);
        }
      });
    }
    STAMP2(19); BAR(); STAMP2(20);
    f32x4 yr[2], yi[2];
    auto turn = [&](int ta, float sg, float (&a)[4], float (&b)[4]) {
#pragma unroll
      for (int e = 0; e < 4; e++) {
        float x = yr[ta][e] * twc[ta][e], y = yi[ta][e] * twc[ta][e];
        pin(x); pin(y);
        a[e] = __builtin_fmaf(sg * yi[ta][e], tws[ta][e], x); b[e] = __builtin_fmaf(-sg * yr[ta][e], tws[ta][e], y);
        pin(a[e]); pin(b[e]);
      }
    };
    stage(6, [&](int i) {
      if (i == 1) {
        wait_c<8>(nb); pin(xa[0][0]); pin(xa[1][0]); pin(wv[0]); pin(wv[1]);
#pragma unroll
        for (int ta = 0; ta < 2; ta++) *(LDSP u32x4*)(luw + ta * 1024) = xa[ta][0];
        lds_arrive(lds + L_CN + CN_X, lane);
      }
      if (i == 2) {
        lds_wait(lds + L_CN + CN_X, 4);
#pragma unroll
        for (int j = 0; j < 8; j++) xa[j & 1][j >> 1] = lds128(lu + j * 1024);
      }
      if (i == 3 || i == 4) {
        const int ta = i - 3;
        PRIO(3);
        yr[ta] = yi[ta] = z4;
#pragma unroll
        for (int j = 0; j < 4; j++) { yr[ta] = mfma(xa[ta][j], bre[j], yr[ta]); yi[ta] = mfma(xa[ta][j], bim[j], yi[ta]); }
        SB(); PRIO(0);
      }
      if (i == 5 || i == 7) {
        const int ta = (i - 5) >> 1;
        float a[4], b[4];
        turn(ta, 1.f, a, b);
        pr[2 * ta] = pk2(a[0], a[1]); pr[2 * ta + 1] = pk2(a[2], a[3]); pi[2 * ta] = pk2(b[0], b[1]); pi[2 * ta + 1] = pk2(b[2], b[3]);
        if (ta) { pin(pr); pin(pi); }
      }
      if (i == 9) {
        PRIO(3);
#pragma unroll
        for (int t = 0; t < 2; t++) {
          yr[t] = mfma(c32[t], pr, z4); yi[t] = mfma(c32[t], pi, z4);
          yr[t] = mfma(s32[t], pi, yr[t]); yi[t] = mfma(n32[t], pr, yi[t]);
        }
        SB(); PRIO(0);
      }
    });
    STAMP2(21); wait_v<12>(); BAR(); STAMP2(22);
    const unsigned grv = SY_RG + (128 * rb + 32 * wi + (lane & 31)) * 8, gcv = SY_CG + cb * 8192 + (64 * wi + lane) * 32;
    u32x2 gr = ald64s(rS, grv, 0);
    u32x4 g0 = ald128s(rS, gcv, 0), g1 = ald128s(rS, gcv, 16);
    stage(7, [&](int i) {
      if (i < 4) {
        const int t = i >> 1, e0 = (i & 1) * 2;
        float fr[2], fi[2];
#pragma unroll
        for (int e = 0; e < 2; e++) {
          const float a = bflo(wv[t][e0 + e]), b = bfhi(wv[t][e0 + e]);
          float x = yr[t][e0 + e] * a, y = yi[t][e0 + e] * a;
          pin(x); pin(y);
          fr[e] = __builtin_fmaf(-yi[t][e0 + e], b, x); fi[e] = __builtin_fmaf(yr[t][e0 + e], b, y);
          pin(fr[e]); pin(fi[e]);
        }
        pr[i] = pk2(fr[0], fr[1]); pi[i] = pk2(fi[0], fi[1]);
        if (i == 3) { pin(pr); pin(pi); }
      }
      if (i == 4) {
        PRIO(3);
#pragma unroll
        for (int t = 0; t < 2; t++) {
          yr[t] = mfma(c32[t], pr, z4); yi[t] = mfma(c32[t], pi, z4);
          yr[t] = mfma(n32[t], pi, yr[t]); yi[t] = mfma(s32[t], pr, yi[t]);
        }
        SB(); PRIO(0);
      }
      if (i == 6 || i == 8) {
        const int ta = (i - 6) >> 1;
        float a[4], b[4];
        turn(ta, -1.f, a, b);
#pragma unroll
        for (int e = 0; e < 4; e++) *(LDSP unsigned*)(uw + (16 * ta + e) * UROW) = pk2(a[e], -b[e]);
      }
    });
    STAMP2(23); wait_c<12>(nb); WAIT_L0(); BAR(); STAMP2(24);
    const rsrc_t rD = mk_rsrc(pwd, W1_BYTES);
    const unsigned dv = (lane >> 3) * 1024 + (((lane & 7) ^ (q << 1)) << 4);
    auto dd = [&](int buf, int n) {
#pragma unroll
      for (int p = 0; p < 4; p++) dma16(rD, lds + wi * 12288, bufaddr(buf) + p * 1024, dv, (32 * step(n) + 8 * p) * 1024 + nb * 128);
    };
    {
#pragma unroll
      for (int j = 0; j < 8; j++) xa[j >> 2][j & 3] = lds128(ur + 16 * (j >> 2) * UROW + 64 * (j & 3));
      SB();
      dd(0, 9);
      PRIO(3);
#pragma unroll
      for (int ta = 0; ta < 2; ta++) {
        yr[ta] = yi[ta] = z4;
#pragma unroll
        for (int j = 0; j < 4; j++) { yr[ta] = mfma(bre[j], xa[ta][j], yr[ta]); yi[ta] = mfma(bim[j], xa[ta][j], yi[ta]); }
      }
      SB(); PRIO(0);
      dd(1, 10);
      const float sc = slot == 255 ? 1.f / 1048576.f : 1.f / 524288.f;
#pragma unroll
      for (int ta = 0; ta < 2; ta++) {
        u32x4 o;
#pragma unroll
        for (int e = 0; e < 4; e++) { float a = yr[ta][e] * sc, b = yi[ta][e] * sc; pin(a); pin(b); o[e] = pk2(a, b); }
        st128<SC1>(o, rS, OFF_Z + slot * 8192 + wi * 2048 + q * 512 + c * 16 + ta * 256);
      }
      SB();
      dd(2, 11);
      pin(gr); pin(g0); pin(g1);
#pragma unroll 1
      for (int it = 0; it < SPIN; it++) {
        if (helper || __all(gr[1] == tag && g0[1] == tag && g0[3] == tag && g1[1] == tag && g1[3] == tag)) break;
        __builtin_amdgcn_s_sleep(2);
        asm volatile("buffer_load_dwordx2 %0, %3, %5, 0 offen sc1\n\tbuffer_load_dwordx4 %1, %4, %5, 0 offen sc1\n\t"
                     "buffer_load_dwordx4 %2, %4, %5, 0 offen offset:16 sc1\n\ts_waitcnt vmcnt(0)"
                     : "=&v"(gr), "=&v"(g0), "=&v"(g1) : "v"(grv), "v"(gcv), "s"(rS) : "memory");
      }
      const float sd = bflo(gr[0]), rr = __builtin_amdgcn_rcpf(sd);
      float s1 = -2.302208198f * rr, s2 = rr * rr, s3 = sd * sd;
      pin(s1); pin(s2); pin(s3);
      s2 *= 0.044715f; pin(s2); s2 *= s1; pin(s2);
      *(LDSP f32x4*)(lds + L_ST + (32 * wi + ((lane & 31) ^ (16 * half))) * 16) = f32x4{bfhi(gr[0]), s1, s2, s3};
      *(LDSP float*)(lds + L_CS + (64 * wi + lane) * 4) = add1(add1(asf(g0[0]), asf(g0[2])), add1(asf(g1[0]), asf(g1[2])));
    }
    STAMP2(25); wait_v<14>(); WAIT_L0(); BAR();
    STAMP(2); CLK(29);
    u32x4 Bq[5][4];
    const unsigned tb = tbase();
    dd(3, 12); dd(4, 13);
    wait_v<12>();
    st32s(tag, rS, lane == 0 ? SY_FZ + wi * 1024 + slot * 4 : OOB);
    STAMP(3);
    dd(5, 0);
    wait_v<13>();
#pragma unroll
    for (int j = 0; j < 3; j++) ldb(Bq[j], tb, j);
    WAIT_L0();
#pragma unroll
    for (int j = 0; j < 12; j++) pin(Bq[j >> 2][j & 3]);
    dd(0, 1); dd(1, 2); dd(2, 3);
    wait_v<16>();
#pragma unroll
    for (int j = 3; j < 5; j++) ldb(Bq[j], tb, j);
    WAIT_L0();
#pragma unroll
    for (int j = 12; j < 20; j++) pin(Bq[j >> 2][j & 3]);
    dd(3, 4); dd(4, 5);
    STAMP(4);
    {
      const unsigned fv = SY_FZ + (rb >> 2) * 1024 + (64 * wi + lane) * 4;
      unsigned zf = ald32s(rS, fv, 0);
      lds_wait(lds + L_CN + CN_S, 4);
      dd(6, 6); dd(7, 7);
      wait_v<8>(); pin(zf);
      if (!__all(zf == tag)) {
        if (helper) __builtin_amdgcn_s_sleep(ZDLY);
        poll<6>(rS, fv, tag);
      }
      lds_set(cLM, 1);
    }
    STAMP(5);
    dd(8, 8);
    res = ald128(rX, obase());
    unsigned hv[4];
#pragma unroll
    for (int p = 0; p < 4; p++) { hv[p] = hbase(p); pin(hv[p]); }
    STAMP(6);
    lds_wait(cML, 1);
    if (helper) __builtin_amdgcn_s_sleep(HDLY);
    __builtin_amdgcn_s_sleep(PDLY);
    STAMP(7);
    poll<0>(rS, lane < 14 ? SY_FH + (rb * 4 + wi) * 2048 + lane * 128 : OOB, tag);
    lds_set(cLM, 2);
    STAMP(8);
    {
      u32x4 A[4][5];
#pragma unroll
      for (int p = 0; p < 4; p++)
#pragma unroll
        for (int j = 0; j < 5; j++) A[p][j] = ald128s(rS, hv[p], step(9 + j) * 4096);
      SB();
      f32x4 a2[4][4];
#pragma unroll
      for (int p = 0; p < 4; p++) {
#pragma unroll
        for (int j = 0; j < 5; j++) {
          const int n = 19 - 5 * p - j;
          asm volatile("s_waitcnt vmcnt(%0)" :: "i"(n) : "memory");
          pin(A[p][j]);
#pragma unroll
          for (int v = 0; v < 4; v++) a2[p][v] = mfma(Bq[j][v], A[p][j], j ? a2[p][v] : z4);
          SB();
          if (p == 1 && j == 0) lds_wait(cML, 2);
          if (p && j < 4) *(LDSP f32x4*)(lds + wi * 12288 + bufaddr(p == 1 ? 5 : p - 2) + j * 1024 + lane * 16) = a2[p - 1][j];
          SB();
        }
      }
#pragma unroll
      for (int v = 0; v < 2; v++) *(LDSP f32x4*)(lds + wi * 12288 + bufaddr(2) + v * 1024 + lane * 16) = a2[3][v];
      o0 = a2[3][2]; o1 = a2[3][3];
    }
    STAMP(9); CLK(30);
  }
  pin(res);
  WAIT_L0();
  STAMP(10);
  BAR();
  STAMP(11);
  if (bid == 0 && wave == 0) st32s(tag, rS, lane == 0 ? SY_E : OOB);
  {
    const unsigned ra = lds + (wave >> 2) * 2048 + lane * 16;
    const unsigned a = ra + wi * 12288 + bufaddr(2);
    o0 += ldsf4(a); o1 += ldsf4(a + 1024);
#pragma unroll
    for (int y = 1; y < 4; y++) {
      const int p = 3 ^ y;
      const unsigned b = ra + (wi ^ y) * 12288, bm = bufaddr(p == 2 ? 6 : 3 + p), bl = bufaddr(p == 0 ? 5 : p - 1);
      o0 += ldsf4(b + bm) + ldsf4(b + bl); o1 += ldsf4(b + bm + 1024) + ldsf4(b + bl + 1024);
    }
    float lo[4], hi[4];
#pragma unroll
    for (int e = 0; e < 4; e++) { const auto r = __builtin_amdgcn_permlane16_swap(asu(o0[e]), asu(o1[e]), false, false); lo[e] = asf(r[0]); hi[e] = asf(r[1]); }
    st128<0>(u32x4{pk2(lo[0] + bflo(res[0]), lo[1] + bfhi(res[0])), pk2(lo[2] + bflo(res[1]), lo[3] + bfhi(res[1])),
                   pk2(hi[0] + bflo(res[2]), hi[1] + bfhi(res[2])), pk2(hi[2] + bflo(res[3]), hi[3] + bfhi(res[3]))}, mk_rsrc(pout, X_BYTES), obase());
  }
  STAMP(12);
  FLUSH();
}

extern "C" void kernel_launch(void* const* d_in, const int* in_sizes, int n_in, void* d_out, int out_size, void* d_ws, size_t ws_size,
                              hipStream_t stream) {
  (void)in_sizes; (void)n_in; (void)out_size; (void)ws_size;
  (void)hipFuncSetAttribute((const void*)fused, hipFuncAttributeMaxDynamicSharedMemorySize, LDS_BYTES);
  fused<<<256, 512, LDS_BYTES, stream>>>(d_in[0], d_in[1], d_in[2], d_in[4], d_in[5], d_in[6], (char*)d_ws, d_out);
}
```

```cpp
#define NOTOUCH 1
#define OAUX 16
#define PROBE 99
#define PROBEW 0
#define PROBEC 0
#include <hip/hip_runtime.h>
#include <hip/hip_bf16.h>
#pragma clang diagnostic ignored "-Wint-to-pointer-cast"

typedef __bf16 bf16x8 __attribute__((ext_vector_type(8)));
typedef __bf16 bf16x2 __attribute__((ext_vector_type(2)));
typedef short s16x4 __attribute__((ext_vector_type(4)));
typedef float f32x4 __attribute__((ext_vector_type(4)));
typedef float f32x2 __attribute__((ext_vector_type(2)));
typedef unsigned u32x4 __attribute__((ext_vector_type(4)));
typedef unsigned u32x2 __attribute__((ext_vector_type(2)));
#define LDSP __attribute__((address_space(3)))
#define DI __device__ __forceinline__
typedef __amdgpu_buffer_rsrc_t rsrc_t;

constexpr unsigned X_BYTES = 2048u * 512u * 2u;
constexpr unsigned W1_BYTES = 512u * 1792u * 2u;
constexpr unsigned WF_BYTES = 2048u * 257u * 2u;
constexpr unsigned HB = 56u * 4096u, OFF_T = 32u * HB, OFF_Z = OFF_T + 256u * 8192u, OFF_WT = OFF_Z + 256u * 8192u, OFF_SY = OFF_WT + 256u * 8192u;
constexpr unsigned SY_E = OFF_SY, SY_FT = OFF_SY + 4096, SY_FZ = OFF_SY + 8192, SY_RG = OFF_SY + 16384,
                   SY_CG = OFF_SY + 32768, SY_FH = SY_CG + 14u * 8192u, WS_BYTES = SY_FH + 64u * 2048u;
constexpr unsigned OOB = 0x40000000u;
constexpr int SC1 = 16;
#define SPINV (1 << 20)
constexpr int SPIN = SPINV;
#define TSTART 4
#define TDEP 1
#define FDLY 8
#define WIN 3
#define PDLY 6
#define HDLY 24
#define ZDLY 48
constexpr unsigned STG = 49152, L_U = 3 * STG, L_ST = L_U + 8704, L_CS = L_ST + 2048, L_CN = L_CS + 1024, L_DG = L_CN + 512, LDS_BYTES = 163840;
constexpr unsigned CN_X = 0, CN_S = 4, CN_LM = 16, CN_ML = 32;
constexpr unsigned UROW = 272, FRUN = 8224;

DI rsrc_t mk_rsrc(const void* p, unsigned bytes) { return __builtin_amdgcn_make_buffer_rsrc((void*)p, 0, bytes, 0x00020000); }
DI unsigned pk2(float a, float b) { bf16x2 v = {(__bf16)a, (__bf16)b}; return __builtin_bit_cast(unsigned, v); }
DI float bflo(unsigned u) { return __builtin_bit_cast(float, u << 16); }
DI float bfhi(unsigned u) { return __builtin_bit_cast(float, u & 0xffff0000u); }
DI float rbf(float a) { return (float)(__bf16)a; }
DI float asf(unsigned u) { return __builtin_bit_cast(float, u); }
DI unsigned asu(float f) { return __builtin_bit_cast(unsigned, f); }
DI f32x4 mfma(u32x4 a, u32x4 b, f32x4 c) {
  return __builtin_amdgcn_mfma_f32_16x16x32_bf16(__builtin_bit_cast(bf16x8, a), __builtin_bit_cast(bf16x8, b), c, 0, 0, 0);
}
DI float cosr(float rev) { return __builtin_amdgcn_cosf(rev); }
DI float sinr(float rev) { return __builtin_amdgcn_sinf(rev); }
template <int N> DI void wait_v() { asm volatile("s_waitcnt vmcnt(%0)" :: "i"(N) : "memory"); }
#define WAIT_L0() asm volatile("s_waitcnt lgkmcnt(0)" ::: "memory")
#define BAR() do { asm volatile("" ::: "memory"); __builtin_amdgcn_s_barrier(); asm volatile("" ::: "memory"); } while (0)
#define SB() __builtin_amdgcn_sched_barrier(0)
template <class T> DI void pin(T& v) { asm volatile("" : "+v"(v)); }
DI void dma16(rsrc_t r, unsigned la, unsigned li, unsigned voff, unsigned soff) {
  asm volatile("s_add_i32 m0, %0, %4\n\ts_nop 0\n\tbuffer_load_dwordx4 %1, %2, %3 offen lds" :: "s"(la), "v"(voff), "s"(r), "s"(soff), "i"(li) : "memory", "scc");
}
DI u32x4 ald128s(rsrc_t r, unsigned v, unsigned s) { u32x4 o; asm volatile("buffer_load_dwordx4 %0, %1, %2, %3 offen sc1" : "=v"(o) : "v"(v), "s"(r), "s"(s) : "memory"); return o; }
DI u32x2 ald64s(rsrc_t r, unsigned v, unsigned s) { u32x2 o; asm volatile("buffer_load_dwordx2 %0, %1, %2, %3 offen sc1" : "=v"(o) : "v"(v), "s"(r), "s"(s) : "memory"); return o; }
DI unsigned ald32s(rsrc_t r, unsigned v, unsigned s) { unsigned o; asm volatile("buffer_load_dword %0, %1, %2, %3 offen sc1" : "=v"(o) : "v"(v), "s"(r), "s"(s) : "memory"); return o; }
DI u32x4 ld128s(rsrc_t r, unsigned v, unsigned s) { return __builtin_amdgcn_raw_buffer_load_b128(r, v, s, SC1); }
template <int AUX> DI void st128(u32x4 v, rsrc_t r, unsigned voff) { __builtin_amdgcn_raw_buffer_store_b128(v, r, voff, 0, AUX); }
DI void st64s(u32x2 v, rsrc_t r, unsigned voff) { __builtin_amdgcn_raw_buffer_store_b64(v, r, voff, 0, SC1); }
DI void st32s(unsigned v, rsrc_t r, unsigned voff) { __builtin_amdgcn_raw_buffer_store_b32(v, r, voff, 0, SC1); }
DI u32x4 lds128(unsigned a) { return *(LDSP u32x4*)a; }
DI f32x4 ldsf4(unsigned a) { return *(LDSP f32x4*)a; }
DI u32x2 ldstr(unsigned a) { return __builtin_bit_cast(u32x2, __builtin_amdgcn_ds_read_tr16_b64_v4i16((LDSP s16x4*)a)); }
template <int D> DI u32x4 ldstr2(unsigned a) { const u32x2 lo = ldstr(a), hi = ldstr(a + D); return u32x4{lo[0], lo[1], hi[0], hi[1]}; }
template <int C> DI float dpp_add(float v) { return v + asf(__builtin_amdgcn_update_dpp(0, asu(v), C, 0xf, 0xf, true)); }
DI float swap16_add(float a, float b) { const auto r = __builtin_amdgcn_permlane16_swap(asu(a), asu(b), false, false); return asf(r[0]) + asf(r[1]); }
DI float swap32_add(float a, float b) { const auto r = __builtin_amdgcn_permlane32_swap(asu(a), asu(b), false, false); return asf(r[0]) + asf(r[1]); }
DI float sum16(float v) { v = dpp_add<0xB1>(v); v = dpp_add<0x4E>(v); v = dpp_add<0x141>(v); return dpp_add<0x128>(v); }
DI float dot2(unsigned a, unsigned b, float s) { return __builtin_amdgcn_fdot2_f32_bf16(__builtin_bit_cast(bf16x2, a), __builtin_bit_cast(bf16x2, b), s, false); }
DI void swp(u32x4& a, u32x4& b) {
#pragma unroll
  for (int e = 0; e < 4; e++) { unsigned x = a[e], y = b[e]; asm volatile("v_swap_b32 %0, %1" : "+v"(x), "+v"(y)); a[e] = x; b[e] = y; }
}
DI float add1(float a, float b) { float o; asm("v_add_f32 %0, %1, %2" : "=v"(o) : "v"(a), "v"(b)); return o; }
template <int NAP> DI void poll(rsrc_t r, unsigned voff, unsigned tag) {
#pragma unroll 1
  for (int it = 0; it < SPIN; it++) {
    unsigned v = ald32s(r, voff, 0);
    wait_v<0>(); pin(v);
    if (__all(voff == OOB || v == tag)) break;
    __builtin_amdgcn_s_sleep(NAP);
  }
}
DI void lds_wait(unsigned a, unsigned n) {
#pragma unroll 1
  for (int it = 0; it < SPIN * 8; it++) { if (*(volatile LDSP unsigned*)a >= n) break; __builtin_amdgcn_s_sleep(1); }
  asm volatile("" ::: "memory");
}
DI void lds_arrive(unsigned a, unsigned lane) {
  if (lane == 0) __hip_atomic_fetch_add((LDSP unsigned*)a, 1u, __ATOMIC_RELAXED, __HIP_MEMORY_SCOPE_WORKGROUP);
  asm volatile("" ::: "memory");
}
DI void lds_set(unsigned a, unsigned v) { asm volatile("" ::: "memory"); *(volatile LDSP unsigned*)a = v; asm volatile("" ::: "memory"); }
DI void dma16n(rsrc_t r, unsigned off, unsigned voff, unsigned soff) {
  asm volatile("buffer_load_dwordx4 %0, %1, %2 offen offset:%3 lds" :: "v"(voff), "s"(r), "s"(soff), "i"(off) : "memory");
}
template <bool V> struct Flag { static constexpr bool v = V; };
DI u32x4 ald128(rsrc_t r, unsigned v) { u32x4 o; asm volatile("buffer_load_dwordx4 %0, %1, %2, 0 offen" : "=v"(o) : "v"(v), "s"(r) : "memory"); return o; }
constexpr unsigned bufaddr(int b) { return (b < 3 ? 2 * STG : b < 6 ? 0 : STG) + (b % 3) * 4096; }
#define PRIO(n) __builtin_amdgcn_s_setprio(n)

#define STAMP(i) do { if ((i) == 0) { pt0 = ptp = (unsigned)__builtin_amdgcn_s_memrealtime(); pc0 = (unsigned)__builtin_amdgcn_s_memtime(); } \
  else if ((i) == PROBE && (wave >> 2) == PROBEW) ptp = PROBEC ? pt0 + ((unsigned)__builtin_amdgcn_s_memtime() - pc0) / 20 : (unsigned)__builtin_amdgcn_s_memrealtime(); } while (0)
#define CLK(i)
#define FLUSH() do { } while (0)
#define STAMP2(i)

__global__ __launch_bounds__(512, 2) void fused(const void* __restrict__ px, const void* __restrict__ pwr, const void* __restrict__ pwi,
                                                const void* __restrict__ pwg, const void* __restrict__ pwu, const void* __restrict__ pwd,
                                                char* ws, void* __restrict__ pout) {
  extern __shared__ __attribute__((aligned(16))) char lds_[];
  const unsigned lds = (unsigned)(size_t)(LDSP char*)lds_;
  asm volatile("" :: "s"(px), "s"(pwr), "s"(pwi), "s"(pwg), "s"(pwu), "s"(ws));
  const int bid = blockIdx.x, xcd = bid & 7, idx = bid >> 3;
  const int rb = (xcd & 3) * 4 + (idx & 3), half = xcd >> 2, nb = idx >> 2, cb = (7 * half + nb) % 14;
  const int pid = 16 * rb + 8 * half + nb, slot = 32 * xcd + idx;
  const bool helper = nb == 7;
  const int wave = __builtin_amdgcn_readfirstlane(threadIdx.x >> 6), wi = wave & 3;
  const unsigned lane = threadIdx.x & 63, c = lane & 15, q = lane >> 4;
  const rsrc_t rS = mk_rsrc(ws, WS_BYTES), rX = mk_rsrc(px, X_BYTES);
  const f32x4 z4 = {0.f, 0.f, 0.f, 0.f};
  unsigned tag;
  unsigned pt0, ptp, pc0;
  STAMP(0);

  auto step = [&](int n) { const int t = cb + n; return 4 * (t >= 14 ? t - 14 : t) + wi; };
  auto tbase = [&]() { return lds + wi * 12288 + (4 * q + (c >> 2)) * 128 + (((2 * q + (c >> 3)) & 3) << 5) + (c & 3) * 8; };
  auto ldb = [&](u32x4 (&B)[4], unsigned tb, int buf) {
#pragma unroll
    for (int v = 0; v < 4; v++) B[v] = ldstr2<2048>((tb ^ (v << 5)) + bufaddr(buf));
  };
  auto hbase = [&](int p) { return (2 * rb + half) * HB + ((p ^ 3 ^ wi) << 10) + lane * 16; };
  auto obase = [&]() { return (128 * rb + 32 * wi + 16 * half + c) * 1024 + nb * 128 + (wave >> 2) * 64 + (q & 1) * 32 + (q >> 1) * 16; };
  const unsigned cLM = lds + L_CN + CN_LM + wi * 4, cML = lds + L_CN + CN_ML + wi * 4;
  f32x4 o0, o1;
  u32x4 res;

  if (wave < 4) {
    f32x4 acc[8][4];
    {
      const unsigned row0 = 128 * rb + 64 * (wi >> 1) + 16 * half + 2 * nb + (wi & 1), ra = lds + 2 * STG + wi * 2048;
      dma16(rX, ra, 0, lane * 16, row0 * 1024);
      dma16(rX, ra, 1024, lane * 16, row0 * 1024 + 32768);
      unsigned ev = ald32s(rS, SY_E, 0);
      if (wave == 0 && lane < 16) *(LDSP unsigned*)(lds + L_CN + lane * 4) = 0;
      u32x4 C[2], N[2], A2;
      f32x4 tc[2], ts[2];
      {
        float co[4], si[4], ca[4], sa[4];
#pragma unroll
        for (int e = 0; e < 4; e++) {
          const float n1 = (float)(4 * q + e), a = n1 * (float)(c + 1);
          co[e] = cosr(a * (1.f / 32.f)); si[e] = -sinr(a * (1.f / 32.f));
          tc[0][e] = cosr(a * (1.f / 512.f)); ts[0][e] = sinr(a * (1.f / 512.f));
          tc[1][e] = cosr(a * (1.f / 512.f) + n1 * (1.f / 32.f)); ts[1][e] = sinr(a * (1.f / 512.f) + n1 * (1.f / 32.f));
          const float b = n1 * (float)(c >> 1) * (1.f / 16.f) + (float)(c & 1) * 0.25f;
          ca[e] = cosr(b); sa[e] = sinr(b);
        }
        const unsigned sg = (c & 1) ? 0u : 0x80008000u;
        const unsigned c01 = pk2(co[0], co[1]), c23 = pk2(co[2], co[3]), s01 = pk2(si[0], si[1]), s23 = pk2(si[2], si[3]);
        C[0] = u32x4{c01, c23, c01 ^ sg, c23 ^ sg}; N[0] = u32x4{s01, s23, s01 ^ sg, s23 ^ sg};
        C[1] = C[0] ^ 0x80000000u; N[1] = N[0] ^ 0x80000000u;
        A2 = u32x4{pk2(ca[0], ca[1]), pk2(ca[2], ca[3]), pk2(sa[0], sa[1]), pk2(sa[2], sa[3])};
        pin(C[0]); pin(C[1]); pin(N[0]); pin(N[1]); pin(A2); pin(tc[0]); pin(tc[1]); pin(ts[0]); pin(ts[1]);
      }
#pragma unroll
      for (int t = 0; t < 8; t++)
#pragma unroll
        for (int m = 0; m < 4; m++) { acc[t][m] = z4; pin(acc[t][m]); }
      wait_v<0>();
      pin(ev);
      tag = __builtin_amdgcn_readfirstlane(ev) + 1;
      STAMP(13);
      u32x4 X[2];
      float s[2], p[2];
#pragma unroll
      for (int n = 0; n < 2; n++) X[n] = ldstr2<512>(ra + n * 1024 + lane * 8);
#pragma unroll
      for (int n = 0; n < 2; n++) {
        s[n] = p[n] = 0.f;
#pragma unroll
        for (int e = 0; e < 4; e++) { s[n] = dot2(X[n][e], 0x3f803f80u, s[n]); p[n] = dot2(X[n][e], X[n][e], p[n]); }
      }
      const float sp = sum16(swap16_add(swap32_add(s[0], s[1]), swap32_add(p[0], p[1])));
      const auto w = __builtin_amdgcn_permlane16_swap(asu(sp), asu(sp), false, false);
      const float m = asf(w[0]) * (1.f / 512.f), var = fmaxf(asf(w[1]) * (1.f / 512.f) - m * m, 0.f);
      const unsigned st = pk2(__builtin_amdgcn_sqrtf(rbf(rbf(var) + 1e-5f)), m);
      st64s(u32x2{st, tag}, rS, (lane & 31) ? OOB : SY_RG + (row0 + lane) * 8);
      const unsigned mb = st & 0xffff0000u, rr = asu(__builtin_amdgcn_rcpf(bflo(st)));
      const float mk = c == 15 ? -32.f : 0.f;
      u32x2 o[4];
#pragma unroll
      for (int n = 0; n < 2; n++) {
        const float mu = asf(__builtin_amdgcn_readlane(mb, 32 * n)), rs = asf(__builtin_amdgcn_readlane(rr, 32 * n));
#pragma unroll
        for (int h = 0; h < 2; h++) {
          f32x4 yr = mfma(X[n], C[h], z4);
          const f32x4 yi = mfma(X[n], N[h], z4);
          if (h) yr += mk * mu;
          const f32x4 a = yr * tc[h] + yi * ts[h], b = yi * tc[h] - yr * ts[h];
          const f32x4 t = mfma(A2, u32x4{pk2(a[0], a[1]), pk2(a[2], a[3]), pk2(b[0], b[1]), pk2(b[2], b[3])}, z4) * rs;
          o[h][n] = pk2(t[0], t[1]); o[2 + h][n] = pk2(t[2], t[3]);
        }
      }
      unsigned ta = lds + L_U + (64 * q + c) * 32 + (wi & 1) * 16 + (wi >> 1) * 8;
      pin(ta);
#pragma unroll
      for (int u = 0; u < 4; u++) *(LDSP u32x2*)(ta + 512 * u) = o[u];
      STAMP(14);
    }
    u32x4 A[9][4];
    if (!helper) {
      PRIO(2);
      unsigned xa0 = lds + c * 128 + ((q ^ (c >> 1)) << 4), xa1 = xa0 ^ 64;
      const unsigned key = (c >> 2) | ((q & 1) << 2);
      unsigned wa0 = lds + 16384 + (8 * q + (c >> 2)) * 256 + (((2 * wi) ^ key) << 5) + (c & 3) * 8, wa1 = wa0 ^ 32;
      u32x4 W[2][4], X[2][4];
      auto lw = [&](int b, int m0, int ks) {
#pragma unroll
        for (int m = m0; m < m0 + 2; m++) W[b][m] = ldstr2<1024>(((m & 1) ? wa1 : wa0) + ks * 8192 + (m >> 1) * 16384);
      };
      auto lx = [&](int h, int ks) {
#pragma unroll
        for (int t = 0; t < 4; t++) X[h][t] = lds128((ks ? xa1 : xa0) + (h * 4 + t) * 2048);
      };
      auto mm = [&](int b, int h, int t0, int t1) {
#pragma unroll
        for (int t = t0; t < t1; t++)
#pragma unroll
          for (int m = 0; m < 4; m++) acc[h * 4 + t][m] = mfma(W[b][m], X[h][t], acc[h * 4 + t][m]);
#pragma unroll
        for (int i = 0; i < (t1 - t0) * 4; i++) { __builtin_amdgcn_sched_group_barrier(0x008, 1, 0); __builtin_amdgcn_sched_group_barrier(0x102, 1, 0); }
        SB();
      };
      WAIT_L0(); BAR();
      STAMP(1); CLK(29);
      lw(0, 0, 0); lw(0, 2, 0); lx(0, 0);
      int sl = 0;
#pragma unroll 1
      for (int s = 0; s < 8; s++) {
        const unsigned dl = (sl == 2) ? -2 * STG : STG;
        sl = (sl == 2) ? 0 : sl + 1;
        lx(1, 0); lw(1, 0, 1); mm(0, 0, 0, 4);
        lw(1, 2, 1); lx(0, 1); mm(0, 1, 0, 4);
        lx(1, 1); xa0 += dl; xa1 += dl; wa0 += dl; wa1 += dl; mm(1, 0, 0, 3);
        pin(xa0); pin(xa1); pin(wa0); pin(wa1);
        STAMP2(15 + s);
        asm volatile("s_waitcnt lgkmcnt(0)\n\ts_cmp_eq_u32 %0, 7\n\ts_cbranch_scc1 1f\n\ts_barrier\n1:" :: "s"(s) : "memory", "scc");
        lw(0, 0, 0); mm(1, 0, 3, 4);
        lw(0, 2, 0); lx(0, 0); mm(1, 1, 0, 4);
      }
      lds_arrive(lds + L_CN + CN_S, lane);
      STAMP(2); CLK(30);
      f32x4 cs[4], st[8];
      unsigned ac = lds + L_CS + 128 * wi + 16 * q, as = lds + L_ST + c * 16;
      pin(ac); pin(as);
      st[0] = ldsf4(as);
#pragma unroll
      for (int m = 0; m < 4; m++) cs[m] = ldsf4(ac + (m >> 1) * 512 + 64 * (m & 1));
#pragma unroll
      for (int t = 1; t < 8; t++) st[t] = ldsf4(as + 256 * t);
      SB();
      const unsigned ho = (cb * 4 + wi) * 4096 + lane * 16;
      const unsigned ho0 = ho + (2 * rb + half) * HB, ho1 = ho + (2 * rb + 1 - half) * HB;
#pragma unroll
      for (int t = 0; t < 8; t++) {
        f32x2 g[4], u[4], y[4];
#pragma unroll
        for (int p = 0; p < 4; p++) { const int v = p >> 1, e = (p & 1) * 2; g[p] = f32x2{acc[t][v][e], acc[t][v][e + 1]} - st[t][0] * f32x2{cs[v][e], cs[v][e + 1]}; }
#pragma unroll
        for (int p = 0; p < 4; p++) y[p] = g[p] * g[p];
        SB();
#pragma unroll
        for (int p = 0; p < 4; p++) y[p] = st[t][1] + st[t][2] * y[p];
#pragma unroll
        for (int p = 0; p < 4; p++) y[p] *= g[p];
        SB();
#pragma unroll
        for (int p = 0; p < 4; p++) {
          const int v = p >> 1, e = (p & 1) * 2;
          y[p] = f32x2{__builtin_amdgcn_exp2f(y[p][0]), __builtin_amdgcn_exp2f(y[p][1])};
          u[p] = f32x2{acc[t][2 + v][e], acc[t][2 + v][e + 1]} - st[t][0] * f32x2{cs[2 + v][e], cs[2 + v][e + 1]};
        }
        SB();
#pragma unroll
        for (int p = 0; p < 4; p++) y[p] = st[t][3] + st[t][3] * y[p];
        SB();
#pragma unroll
        for (int p = 0; p < 4; p++) { y[p] = f32x2{__builtin_amdgcn_rcpf(y[p][0]), __builtin_amdgcn_rcpf(y[p][1])}; u[p] *= g[p]; }
        SB();
#pragma unroll
        for (int p = 0; p < 4; p++) u[p] *= y[p];
        SB();
        const u32x4 o = {pk2(u[0][0], u[0][1]), pk2(u[1][0], u[1][1]), pk2(u[2][0], u[2][1]), pk2(u[3][0], u[3][1])};
        st128<SC1>(o, rS, ((t & 1) ? ho1 : ho0) + (t >> 1) * 1024);
        if (!(t & 1)) A[0][t >> 1] = o;
      }
      SB();
    } else {
      WAIT_L0(); BAR();
      lds_arrive(lds + L_CN + CN_S, lane);
#pragma unroll
      for (int p = 0; p < 4; p++) A[0][p] = u32x4{0, 0, 0, 0};
    }
    STAMP(3);
    lds_wait(cLM, 1);
    STAMP(4);
    f32x4 a2[4][4];
    {
      u32x4 G[4][4], Z[16];
      const unsigned zv = OFF_Z + 16 * q * 8192 + rb * 512 + (16 * half + c) * 16;
#pragma unroll
      for (int v = 0; v < 4; v++) {
        Z[4 * v] = ald128s(rS, zv, (64 * wi + 4 * v) * 8192);
        SB();
        const float d = (float)(64 * nb + 16 * v + c) * (1.f / 512.f), a = d * (float)(64 * wi + 16 * q);
        f32x2 u0 = {cosr(a), sinr(a)}, u1 = {cosr(a + d), sinr(a + d)};
        const float k = 2.f * cosr(d);
#pragma unroll
        for (int j = 0; j < 16; j++) {
          G[j >> 2][v][j & 3] = pk2(u1[0], u1[1]);
          const f32x2 u2 = k * u1 - u0; u0 = u1; u1 = u2;
          if ((j & 3) == 3) { pin(G[j >> 2][v]); if (j < 12) Z[4 * v + 1 + (j >> 2)] = ald128s(rS, zv, (64 * wi + 4 * v + 1 + (j >> 2)) * 8192); SB(); }
        }
        if (v == 2) {
          wait_v<12>();
          st32s(tag, rS, (lane == 0 && !helper) ? SY_FH + (rb * 4 + wi) * 2048 + cb * 128 : OOB);
          lds_set(cML, 1);
          STAMP(5);
        }
      }
      res = ald128(rX, obase());
      if (!(wi & 1)) { swp(A[0][0], A[0][1]); swp(A[0][2], A[0][3]); }
      if (!(wi & 2)) { swp(A[0][0], A[0][2]); swp(A[0][1], A[0][3]); }
      STAMP(6);
#pragma unroll
      for (int zz = 0; zz < 4; zz++) {
        if (zz == 0) wait_v<6>();
        if (zz == 3) wait_v<1>();
#pragma unroll
        for (int j = 0; j < 4; j++) pin(Z[4 * zz + j]);
#pragma unroll
        for (int p = 0; p < 4; p++) {
          const u32x4 zf = {Z[4 * zz][p], Z[4 * zz + 1][p], Z[4 * zz + 2][p], Z[4 * zz + 3][p]};
#pragma unroll
          for (int v = 0; v < 4; v++) a2[p][v] = mfma(G[zz][v], zf, zz ? a2[p][v] : z4);
        }
        SB();
      }
    }
    STAMP(7);
    {
      unsigned hv[4];
#pragma unroll
      for (int p = 0; p < 4; p++) { hv[p] = hbase(p); pin(hv[p]); }
      const unsigned tb = tbase();
      constexpr int bf[9] = {5, 0, 1, 2, 3, 4, 6, 7, 8};
      u32x4 B[2][4];
      lds_wait(cLM, 2);
      STAMP(8);
      ldb(B[0], tb, bf[0]);
      if (helper) {
#pragma unroll
        for (int p = 0; p < 4; p++) asm volatile("buffer_load_dwordx4 %0, %1, %2, %3 offen sc1" : "+v"(A[0][p]) : "v"(hv[p]), "s"(rS), "s"(step(0) * 4096) : "memory");
      }
#pragma unroll
      for (int j = 1; j <= WIN; j++)
#pragma unroll
        for (int p = 0; p < 4; p++) A[j][p] = ald128s(rS, hv[p], step(j) * 4096);
      SB();
#pragma unroll
      for (int j = 0; j < 9; j++) {
        if (j < 8) ldb(B[(j + 1) & 1], tb, bf[j + 1]);
        if (j == 2) lds_set(cML, 2);
        SB();
#pragma unroll
        for (int p = 0; p < 4; p++) {
          if (j + WIN < 8) wait_v<4 * WIN + 3>(); else {
            const int n = 3 - p + 4 * (8 - j);
            asm volatile("s_waitcnt vmcnt(%0)" :: "i"(n) : "memory");
          }
          pin(A[j][p]);
#pragma unroll
          for (int v = 0; v < 4; v++) a2[p][v] = mfma(B[j & 1][v], A[j][p], a2[p][v]);
          SB();
          if (j + WIN < 8) A[j + WIN + 1][p] = ald128s(rS, hv[p], step(j + WIN + 1) * 4096);
          if (j == 8 && p) {
#pragma unroll
            for (int v = 0; v < 4; v++) *(LDSP f32x4*)(lds + wi * 12288 + bufaddr(p == 3 ? 6 : 2 + p) + v * 1024 + lane * 16) = a2[p - 1][v];
          }
          SB();
        }
      }
#pragma unroll
      for (int v = 2; v < 4; v++) *(LDSP f32x4*)(lds + wi * 12288 + bufaddr(2) + v * 1024 + lane * 16) = a2[3][v];
      o0 = a2[3][0]; o1 = a2[3][1];
    }
    STAMP(9);
  } else {
    const unsigned cp = c ^ 3 ^ (slot >> 6), k1 = 16 * wi + cp;
    u32x4 bre[4], bim[4], g0, g1;
    u32x2 gr;
    const unsigned grv = SY_RG + (128 * rb + 32 * wi + (lane & 31)) * 8, gcv = SY_CG + cb * 8192 + (64 * wi + lane) * 32;
    auto front = [&](auto HF) {
      constexpr bool H = decltype(HF)::v;
      const rsrc_t rG = mk_rsrc(pwg, W1_BYTES), rU = mk_rsrc(pwu, W1_BYTES), rB = mk_rsrc((const char*)px - 4096, X_BYTES + 4096);
      unsigned xv[4], wv4[4];
      if (!H) {
#pragma unroll
        for (int i = 0; i < 4; i++) {
          xv[i] = 4096 - 1024 * i + (rb * 128 + ((4 * wi + i) ^ (2 * half)) * 8 + (lane >> 3)) * 1024 + ((((lane & 7) ^ q) << 4) ^ ((i & 1) * 64));
          wv4[i] = (16 * wi + 4 * i + q) * 3584 - 1024 * i + cb * 256 + (((c ^ (q << 1)) << 4) ^ ((i & 2) * 64));
        }
      }
      auto stage = [&](int s, auto&& job) {
        const int ks = (s + 2 * (rb >> 2)) & 7;
#pragma unroll
        for (int i = 0; i < 12; i++) {
          if (!H) {
            const rsrc_t r = i < 4 ? rB : i < 8 ? rG : rU;
            const unsigned vo = i < 4 ? xv[i] : wv4[i & 3], so = i < 4 ? ks * 128 : ks * (64 * 3584);
            if (i & 3) dma16n(r, (i & 3) * 1024, vo, so); else dma16(r, lds + wi * 4096, (s % 3) * STG + (i >> 2) * 16384, vo, so);
          }
          job(i); SB();
        }
      };
      unsigned nx = 0;
      auto meet = [&]() { nx += 4; lds_arrive(lds + L_CN + CN_X, lane); lds_wait(lds + L_CN + CN_X, nx); };
      u32x4 c32[2], s32[2], n32[2];
      float twc[2][4], tws[2][4], co[4], si[4];
      auto cjob = [&](int n) {
        if (n < 4) {
          const float a = (float)(k1 * (4 * q + n)) * (1.f / 64.f), x = cosr(a), y = sinr(a);
          unsigned u = pk2(x, y), v = pk2(-y, x); pin(u); pin(v);
          bre[0][n] = u; bim[0][n] = v;
        } else if (n < 8) {
          const unsigned u = bre[0][n & 3], v = bim[0][n & 3], m2 = (k1 & 1) ? 0x80008000u : 0u, m1 = (k1 & 2) ? 0x80008000u : 0u;
          unsigned u1 = ((k1 & 1) ? v : u) ^ m1, v1 = ((k1 & 1) ? u : v) ^ m1 ^ m2, u2 = u ^ m2, v2 = v ^ m2, u3 = u1 ^ m2, v3 = v1 ^ m2;
          pin(u1); pin(v1); pin(u2); pin(v2); pin(u3); pin(v3);
          bre[1][n & 3] = u1; bim[1][n & 3] = v1; bre[2][n & 3] = u2; bim[2][n & 3] = v2; bre[3][n & 3] = u3; bim[3][n & 3] = v3;
        } else if (n < 16) {
          const float a = (float)(k1 * (16 * ((n >> 2) & 1) + 4 * q + (n & 3))) * (1.f / 2048.f);
          float x = cosr(a), y = sinr(a); pin(x); pin(y);
          twc[(n >> 2) & 1][n & 3] = x; tws[(n >> 2) & 1][n & 3] = y;
        } else if (n < 20) {
          const float a = (float)(c * (4 * q + (n & 3))) * (1.f / 32.f);
          float x = cosr(a), y = sinr(a); pin(x); pin(y);
          co[n & 3] = x; si[n & 3] = y;
        } else if (n == 20) {
          const unsigned sg = (c & 1) ? 0x80008000u : 0u;
          const unsigned c01 = pk2(co[0], co[1]), c23 = pk2(co[2], co[3]), s01 = pk2(si[0], si[1]), s23 = pk2(si[2], si[3]);
          c32[0] = u32x4{c01, c23, c01 ^ sg, c23 ^ sg}; s32[0] = u32x4{s01, s23, s01 ^ sg, s23 ^ sg};
          pin(c32[0]); pin(s32[0]);
        } else if (n == 21) {
          c32[1] = c32[0] ^ 0x80000000u; s32[1] = s32[0] ^ 0x80000000u; pin(c32[1]); pin(s32[1]);
        } else if (n == 22) {
          n32[0] = s32[0] ^ 0x80008000u; n32[1] = s32[1] ^ 0x80008000u; pin(n32[0]); pin(n32[1]);
        }
      };
      const int hid = 2 * rb + half;
      if (H) {
        const unsigned fo = (16 * (hid >> 3) + 256 * (hid & 7) + 64 * wi) * 514;
#pragma unroll
        for (int a = 0; a < 2; a++) {
          const rsrc_t rF = mk_rsrc(a ? pwi : pwr, WF_BYTES);
#pragma unroll
          for (int n = 0; n < 8; n++) {
            if (n & 3) dma16n(rF, (n & 3) * 1024, lane * 16, fo + (n & 4) * 1024); else dma16(rF, lds + 2 * wi * FRUN, a * FRUN + n * 1024, lane * 16, fo + n * 1024);
          }
          if (lane < 2) dma16(rF, lds + 2 * wi * FRUN, a * FRUN + 8192, lane * 16, fo + 8192);
        }
      }
      stage(0, [&](int i) { if (i > 3) cjob(i - 4); });
      stage(1, [&](int i) { cjob(8 + i); });
      unsigned ev = ald32s(rS, SY_E, 0);
      cjob(20); cjob(21); cjob(22);
      unsigned ca = lds + 16384 + (rb & 2) * 8192 + (((c >> 1) & 3) + 8 * (c >> 3) + 4 * (q & 1) + 16 * (q >> 1)) * 256 +
                    (((4 * (rb & 1) + wi) ^ (c >> 1)) << 5) + (c & 1) * 16;
      unsigned cg = (lane & 24) ? OOB : SY_CG + cb * 8192 + (64 * (rb & 3) + 16 * wi + 8 * (lane >> 5) + c) * 32 + (rb >> 2) * 8;
      u32x4 sel, cr[2];
      f32x4 cs = z4;
      auto col_job = [&](int s, int i) {
        if (H) return;
        if (i == 1) { cr[0] = lds128(ca + s * STG); cr[1] = lds128(ca + s * STG + 8192); }
        if (i == 4) { PRIO(3); cs = mfma(cr[0], sel, cs); cs = mfma(cr[1], sel, cs); pin(cs); SB(); PRIO(0); }
      };
      if (!H) {
#pragma unroll
        for (int e = 0; e < 4; e++) sel[e] = ((c >> 1) & 3) == e ? ((c & 1) ? 0x3f800000u : 0x3f80u) : 0u;
        pin(sel); pin(ca); pin(cg);
      }
      unsigned lu = lds + L_U + lane * 16, luw = lu + wi * 2048, uw = lds + L_U + 4 * q * UROW + k1 * 4;
      unsigned to = OFF_T + pid * 8192 + wi * 2048 + lane * 16, fo = lane == 0 ? SY_FT + wi * 1024 + pid * 4 : OOB;
      unsigned fv = SY_FT + (slot >> 6) * 1024 + (64 * wi + lane) * 4;
      unsigned tg = OFF_T + (64 * wi + 16 * q + (c >> 1)) * 8192 + slot * 32 + (c & 1) * 16, wg = OFF_WT + slot * 8192 + wi * 2048 + q * 256 + cp * 16;
      pin(lu); pin(luw); pin(uw); pin(to); pin(fo); pin(fv); pin(tg); pin(wg);
      wait_v<H ? 0 : 13>(); BAR();
      {
        const u32x4 t0 = lds128(luw), t1 = lds128(luw + 1024);
        SB();
        stage(2, [&](int i) {
          if (i == 0) { st128<SC1>(t0, rS, to); st128<SC1>(t1, rS, to + 1024); }
          col_job(0, i);
        });
      }
      if (H) {
        const unsigned od = c & 1, sal = od ? 0x07060504u : 0x05040302u, slo = od ? 0x01000504u : 0x05040100u, shi = slo + 0x02020202u;
#pragma unroll 1
        for (int it = 0; it < 2; it++) {
          const unsigned g = 4 * q + wi + 16 * it;
          const unsigned a0 = lds + c * 514 - 2 * od + 16 * g, aa = a0 + od * FRUN, ab = a0 + FRUN - od * FRUN;
          u32x4 w[8];
#pragma unroll
          for (int e = 0; e < 4; e++) {
            unsigned x[5], y[5];
#pragma unroll
            for (int j = 0; j < 5; j++) { x[j] = *(LDSP unsigned*)(aa + e * 2 * FRUN + j * 4); y[j] = *(LDSP unsigned*)(ab + e * 2 * FRUN + j * 4); }
#pragma unroll
            for (int j = 0; j < 4; j++) {
              const unsigned u = __builtin_amdgcn_perm(x[j + 1], x[j], sal), v = __builtin_amdgcn_perm(y[j + 1], y[j], sal);
              w[2 * j][e] = __builtin_amdgcn_perm(v, u, slo); w[2 * j + 1][e] = __builtin_amdgcn_perm(v, u, shi);
            }
          }
#pragma unroll
          for (int j = 0; j < 8; j++) st128<SC1>(w[j], rS, OFF_WT + (8 * g + j) * 8192 + hid * 256 + c * 16);
        }
      }
      STAMP2(13); wait_v<H ? 0 : 14>(); if (H) meet(); else BAR(); STAMP2(14);
      pin(ev);
      tag = __builtin_amdgcn_readfirstlane(ev) + 1;
      stage(3, [&](int i) {
        if (i == 3) { wait_v<15>(); st32s(tag, rS, fo); }
        col_job(1, i);
      });
      if (!H) {
        STAMP2(15); wait_v<13>(); BAR(); STAMP2(16);
        stage(4, [&](int i) {
          if (i == 0) {
            float v = swap32_add(cs[0] + cs[2], cs[1] + cs[3]);
            v = swap16_add(v, v);
            st64s(u32x2{asu(v), tag}, rS, cg);
          }
        });
        STAMP2(17); wait_v<13>(); BAR(); STAMP2(18);
      } else __builtin_amdgcn_s_sleep(FDLY);
      u32x4 xa[2][4], wv[2], pr, pi;
      {
        unsigned ft = ald32s(rS, fv, 0);
        stage(5, [&](int i) {
          if (i == 5) {
            wait_v<H ? 0 : 6>(); pin(ft);
            if (!__all(ft == tag)) poll<4>(rS, fv, tag);
            STAMP(1);
#pragma unroll
            for (int ta = 0; ta < 2; ta++) xa[ta][0] = ald128s(rS, tg, ta * 65536);
#pragma unroll
            for (int t = 0; t < 2; t++) wv[t] = ald128s(rS, wg, t * 1024);
          }
        });
      }
      STAMP2(19); if (!H) BAR(); STAMP2(20);
      f32x4 yr[2], yi[2];
      auto turn = [&](int ta, int e0, float sg, float (&a)[2], float (&b)[2]) {
        float x[2], y[2];
#pragma unroll
        for (int e = 0; e < 2; e++) { x[e] = yr[ta][e0 + e] * twc[ta][e0 + e]; y[e] = yi[ta][e0 + e] * twc[ta][e0 + e]; pin(x[e]); pin(y[e]); }
#pragma unroll
        for (int e = 0; e < 2; e++) {
          a[e] = __builtin_fmaf(sg * yi[ta][e0 + e], tws[ta][e0 + e], x[e]); b[e] = __builtin_fmaf(-sg * yr[ta][e0 + e], tws[ta][e0 + e], y[e]);
          pin(a[e]); pin(b[e]);
        }
      };
      stage(6, [&](int i) {
        if (i == 1) {
          wait_v<H ? 0 : 8>(); pin(xa[0][0]); pin(xa[1][0]); pin(wv[0]); pin(wv[1]);
#pragma unroll
          for (int ta = 0; ta < 2; ta++) *(LDSP u32x4*)(luw + ta * 1024) = xa[ta][0];
          nx += 4; lds_arrive(lds + L_CN + CN_X, lane);
        }
        if (i == 2) {
          lds_wait(lds + L_CN + CN_X, nx);
#pragma unroll
          for (int j = 0; j < 8; j++) xa[j >> 2][j & 3] = lds128(lu + (j & 3) * 2048 + (j >> 2) * 1024);
        }
        if (i == 4 || i == 5) {
          const int ta = i - 4;
          PRIO(3);
          yr[ta] = yi[ta] = z4;
#pragma unroll
          for (int j = 0; j < 4; j++) { yr[ta] = mfma(xa[ta][j], bre[j], yr[ta]); yi[ta] = mfma(xa[ta][j], bim[j], yi[ta]); }
          SB(); PRIO(0);
        }
        if (i >= 6 && i < 10) {
          float a[2], b[2];
          turn((i - 6) >> 1, (i & 1) * 2, 1.f, a, b);
          pr[i - 6] = pk2(a[0], a[1]); pi[i - 6] = pk2(b[0], b[1]);
          if (i == 9) { pin(pr); pin(pi); }
        }
        if (i == 10) {
          PRIO(3);
#pragma unroll
          for (int t = 0; t < 2; t++) {
            yr[t] = mfma(c32[t], pr, z4); yi[t] = mfma(c32[t], pi, z4);
            yr[t] = mfma(s32[t], pi, yr[t]); yi[t] = mfma(n32[t], pr, yi[t]);
          }
          SB(); PRIO(0);
        }
      });
      STAMP2(21); if (H) meet(); else { wait_v<12>(); BAR(); } STAMP2(22);
      gr = ald64s(rS, grv, 0); g0 = ald128s(rS, gcv, 0); g1 = ald128s(rS, gcv, 16);
      stage(7, [&](int i) {
        if (i < 4) {
          const int t = i >> 1, e0 = (i & 1) * 2;
          float fr[2], fi[2];
#pragma unroll
          for (int e = 0; e < 2; e++) {
            const float a = bflo(wv[t][e0 + e]), b = bfhi(wv[t][e0 + e]);
            float x = yr[t][e0 + e] * a, y = yi[t][e0 + e] * a;
            pin(x); pin(y);
            fr[e] = __builtin_fmaf(-yi[t][e0 + e], b, x); fi[e] = __builtin_fmaf(yr[t][e0 + e], b, y);
            pin(fr[e]); pin(fi[e]);
          }
          pr[i] = pk2(fr[0], fr[1]); pi[i] = pk2(fi[0], fi[1]);
          if (i == 3) { pin(pr); pin(pi); }
        }
        if (i == 4) {
          PRIO(3);
#pragma unroll
          for (int t = 0; t < 2; t++) {
            yr[t] = mfma(c32[t], pr, z4); yi[t] = mfma(c32[t], pi, z4);
            yr[t] = mfma(n32[t], pi, yr[t]); yi[t] = mfma(s32[t], pr, yi[t]);
          }
          SB(); PRIO(0);
        }
        if (i >= 6 && i < 10) {
          const int ta = (i - 6) >> 1, e0 = (i & 1) * 2;
          float a[2], b[2];
          turn(ta, e0, -1.f, a, b);
#pragma unroll
          for (int e = 0; e < 2; e++) *(LDSP unsigned*)(uw + (16 * ta + e0 + e) * UROW) = pk2(a[e], -b[e]);
        }
      });
      STAMP2(23); wait_v<H ? 0 : 12>(); pin(gr); pin(g0); pin(g1);
      if (H) meet(); else { WAIT_L0(); BAR(); } STAMP2(24);
    };
    if (helper) front(Flag<true>()); else front(Flag<false>());
    const rsrc_t rD = mk_rsrc(pwd, W1_BYTES);
    unsigned dv[4];
#pragma unroll
    for (int p = 0; p < 4; p++) dv[p] = (lane >> 3) * 1024 + (((lane & 7) ^ (q << 1)) << 4) + nb * 128 + p * 7168;
    auto dd = [&](int buf, int n) {
      dma16(rD, lds + wi * 12288, bufaddr(buf), dv[0], step(n) * 32768);
#pragma unroll
      for (int p = 1; p < 4; p++) dma16n(rD, p * 1024, dv[p], step(n) * 32768);
    };
    {
      u32x4 xa[2][4];
      f32x4 yr[2], yi[2];
      unsigned ur = lds + L_U + c * UROW + 16 * q;
      pin(ur);
#pragma unroll
      for (int j = 0; j < 8; j++) xa[j >> 2][j & 3] = lds128(ur + 16 * (j >> 2) * UROW + 64 * (j & 3));
      SB();
      dd(0, 9);
      PRIO(3);
#pragma unroll
      for (int ta = 0; ta < 2; ta++) {
        yr[ta] = yi[ta] = z4;
#pragma unroll
        for (int j = 0; j < 4; j++) { yr[ta] = mfma(bre[j], xa[ta][j], yr[ta]); yi[ta] = mfma(bim[j], xa[ta][j], yi[ta]); }
      }
      SB(); PRIO(0);
      dd(1, 10);
      const float sc = slot == 255 ? 1.f / 1048576.f : 1.f / 524288.f;
#pragma unroll
      for (int ta = 0; ta < 2; ta++) {
        u32x4 o;
#pragma unroll
        for (int e = 0; e < 4; e++) { float a = yr[ta][e] * sc, b = yi[ta][e] * sc; pin(a); pin(b); o[e] = pk2(a, b); }
        st128<SC1>(o, rS, OFF_Z + slot * 8192 + wi * 2048 + q * 512 + c * 16 + ta * 256);
      }
      SB();
      dd(2, 11);
#pragma unroll 1
      for (int it = 0; it < SPIN; it++) {
        if (helper || __all(gr[1] == tag && g0[1] == tag && g0[3] == tag && g1[1] == tag && g1[3] == tag)) break;
        __builtin_amdgcn_s_sleep(2);
        asm volatile("buffer_load_dwordx2 %0, %3, %5, 0 offen sc1\n\tbuffer_load_dwordx4 %1, %4, %5, 0 offen sc1\n\t"
                     "buffer_load_dwordx4 %2, %4, %5, 0 offen offset:16 sc1\n\ts_waitcnt vmcnt(0)"
                     : "=&v"(gr), "=&v"(g0), "=&v"(g1) : "v"(grv), "v"(gcv), "s"(rS) : "memory");
      }
      const float sd = bflo(gr[0]), rr = __builtin_amdgcn_rcpf(sd);
      float s1 = -2.302208198f * rr, s2 = rr * rr, s3 = sd * sd;
      pin(s1); pin(s2); pin(s3);
      s2 *= 0.044715f; pin(s2); s2 *= s1; pin(s2);
      *(LDSP f32x4*)(lds + L_ST + (32 * wi + ((lane & 31) ^ (16 * half))) * 16) = f32x4{bfhi(gr[0]), s1, s2, s3};
      *(LDSP float*)(lds + L_CS + (64 * wi + lane) * 4) = add1(add1(asf(g0[0]), asf(g0[2])), add1(asf(g1[0]), asf(g1[2])));
    }
    STAMP2(25); wait_v<14>(); WAIT_L0(); if (!helper) BAR();
    STAMP(2); CLK(29);
    u32x4 Bq[5][4];
    const unsigned tb = tbase();
    dd(3, 12); dd(4, 13);
    wait_v<12>();
    st32s(tag, rS, lane == 0 ? SY_FZ + wi * 1024 + slot * 4 : OOB);
    STAMP(3);
    dd(5, 0);
    wait_v<13>();
#pragma unroll
    for (int j = 0; j < 3; j++) ldb(Bq[j], tb, j);
    WAIT_L0();
#pragma unroll
    for (int j = 0; j < 12; j++) pin(Bq[j >> 2][j & 3]);
    dd(0, 1); dd(1, 2); dd(2, 3);
    wait_v<16>();
#pragma unroll
    for (int j = 3; j < 5; j++) ldb(Bq[j], tb, j);
    WAIT_L0();
#pragma unroll
    for (int j = 12; j < 20; j++) pin(Bq[j >> 2][j & 3]);
    dd(3, 4); dd(4, 5);
    STAMP(4);
    {
      const unsigned fv = SY_FZ + (rb >> 2) * 1024 + (64 * wi + lane) * 4;
      unsigned zf = ald32s(rS, fv, 0);
      lds_wait(lds + L_CN + CN_S, 4);
      dd(6, 6); dd(7, 7);
      wait_v<8>(); pin(zf);
      if (!__all(zf == tag)) {
        if (helper) __builtin_amdgcn_s_sleep(ZDLY);
        poll<6>(rS, fv, tag);
      }
      lds_set(cLM, 1);
    }
    STAMP(5);
    dd(8, 8);
    res = ald128(rX, obase());
    unsigned hv[4];
#pragma unroll
    for (int p = 0; p < 4; p++) { hv[p] = hbase(p); pin(hv[p]); }
    STAMP(6);
    lds_wait(cML, 1);
    if (helper) __builtin_amdgcn_s_sleep(HDLY);
    __builtin_amdgcn_s_sleep(PDLY);
    STAMP(7);
    poll<0>(rS, lane < 14 ? SY_FH + (rb * 4 + wi) * 2048 + lane * 128 : OOB, tag);
    lds_set(cLM, 2);
    STAMP(8);
    {
      u32x4 A[4][5];
#pragma unroll
      for (int p = 0; p < 4; p++)
#pragma unroll
        for (int j = 0; j < 5; j++) A[p][j] = ald128s(rS, hv[p], step(9 + j) * 4096);
      SB();
      f32x4 a2[4][4];
#pragma unroll
      for (int p = 0; p < 4; p++) {
#pragma unroll
        for (int j = 0; j < 5; j++) {
          const int n = 19 - 5 * p - j;
          asm volatile("s_waitcnt vmcnt(%0)" :: "i"(n) : "memory");
          pin(A[p][j]);
#pragma unroll
          for (int v = 0; v < 4; v++) a2[p][v] = mfma(Bq[j][v], A[p][j], j ? a2[p][v] : z4);
          SB();
          if (p == 1 && j == 0) lds_wait(cML, 2);
          if (p && j < 4) *(LDSP f32x4*)(lds + wi * 12288 + bufaddr(p == 1 ? 5 : p - 2) + j * 1024 + lane * 16) = a2[p - 1][j];
          SB();
        }
      }
#pragma unroll
      for (int v = 0; v < 2; v++) *(LDSP f32x4*)(lds + wi * 12288 + bufaddr(2) + v * 1024 + lane * 16) = a2[3][v];
      o0 = a2[3][2]; o1 = a2[3][3];
    }
    STAMP(9); CLK(30);
  }
  pin(res);
  WAIT_L0();
  STAMP(10);
  BAR();
  STAMP(11);
  if (bid == 0 && wave == 0) st32s(tag, rS, lane == 0 ? SY_E : OOB);
  {
    const unsigned ra = lds + (wave >> 2) * 2048 + lane * 16;
    const unsigned a = ra + wi * 12288 + bufaddr(2);
    o0 += ldsf4(a); o1 += ldsf4(a + 1024);
#pragma unroll
    for (int y = 1; y < 4; y++) {
      const int p = 3 ^ y;
      const unsigned b = ra + (wi ^ y) * 12288, bm = bufaddr(p == 2 ? 6 : 3 + p), bl = bufaddr(p == 0 ? 5 : p - 1);
      o0 += ldsf4(b + bm) + ldsf4(b + bl); o1 += ldsf4(b + bm + 1024) + ldsf4(b + bl + 1024);
    }
    float lo[4], hi[4];
#pragma unroll
    for (int e = 0; e < 4; e++) { const auto r = __builtin_amdgcn_permlane16_swap(asu(o0[e]), asu(o1[e]), false, false); lo[e] = asf(r[0]); hi[e] = asf(r[1]); }
    st128<OAUX>(u32x4{pk2(lo[0] + bflo(res[0]), lo[1] + bfhi(res[0])), pk2(lo[2] + bflo(res[1]), lo[3] + bfhi(res[1])),
                   pk2(hi[0] + bflo(res[2]), hi[1] + bfhi(res[2])), pk2(hi[2] + bflo(res[3]), hi[3] + bfhi(res[3]))}, mk_rsrc(pout, X_BYTES), obase());
  }
  STAMP(12);
  FLUSH();
}

__global__ void ka(char* ws) { unsigned t = (unsigned)__builtin_amdgcn_s_memrealtime(); if (threadIdx.x == 0) *(unsigned*)(ws + (72u << 20)) = t; }
__global__ void kc(char* ws) { unsigned t = __builtin_amdgcn_readfirstlane(*(volatile unsigned*)(ws + (72u << 20))); while ((unsigned)__builtin_amdgcn_s_memrealtime() - t < 4000) __builtin_amdgcn_s_sleep(4); }
extern "C" void kernel_launch(void* const* d_in, const int* in_sizes, int n_in, void* d_out, int out_size, void* d_ws, size_t ws_size,
                              hipStream_t stream) {
  (void)in_sizes; (void)n_in; (void)out_size; (void)ws_size;
  (void)hipFuncSetAttribute((const void*)fused, hipFuncAttributeMaxDynamicSharedMemorySize, LDS_BYTES);
  ka<<<1, 64, 0, stream>>>((char*)d_ws);
  fused<<<256, 512, LDS_BYTES, stream>>>(d_in[0], d_in[1], d_in[2], d_in[4], d_in[5], d_in[6], (char*)d_ws, d_out);
  kc<<<1, 64, 0, stream>>>((char*)d_ws);
}
```

```cpp
#include <hip/hip_runtime.h>
#include <hip/hip_bf16.h>
#pragma clang diagnostic ignored "-Wint-to-pointer-cast"

typedef __bf16 bf16x8 __attribute__((ext_vector_type(8)));
typedef __bf16 bf16x2 __attribute__((ext_vector_type(2)));
typedef short s16x4 __attribute__((ext_vector_type(4)));
typedef float f32x4 __attribute__((ext_vector_type(4)));
typedef float f32x2 __attribute__((ext_vector_type(2)));
typedef unsigned u32x4 __attribute__((ext_vector_type(4)));
typedef unsigned u32x2 __attribute__((ext_vector_type(2)));
#define LDSP __attribute__((address_space(3)))
#define DI __device__ __forceinline__
typedef __amdgpu_buffer_rsrc_t rsrc_t;

constexpr unsigned X_BYTES = 2048u * 512u * 2u;
constexpr unsigned W1_BYTES = 512u * 1792u * 2u;
constexpr unsigned WF_BYTES = 2048u * 257u * 2u;
constexpr unsigned HB = 56u * 4096u, OFF_T = 32u * HB, OFF_Z = OFF_T + 256u * 8192u, OFF_WT = OFF_Z + 256u * 8192u, OFF_SY = OFF_WT + 256u * 8192u;
constexpr unsigned SY_E = OFF_SY, SY_FZ = OFF_SY + 8192, SY_RG = OFF_SY + 16384, SY_CG = OFF_SY + 32768, SY_FH = SY_CG + 14u * 8192u,
                   SY_FT = SY_FH + 64u * 2048u, SY_FW = SY_FT + 65536u, WS_BYTES = SY_FW + 32768u;
constexpr unsigned OOB = 0x40000000u;
constexpr int SC1 = 16;
#define SPINV (1 << 20)
constexpr int SPIN = SPINV;
#define OAUX 16
#define FDLY 0
#define WIN 3
#define PDLY 6
#define HDLY 24
#define EXP 0
#define ZNAP 8
#define THR 4
#define THR2 8
#define GV 2
constexpr unsigned STG = 49152, L_U = 3 * STG, L_ST = L_U + 8192, L_CS = L_ST + 2048, L_CN = L_CS + 1024, L_DG = L_CN + 512, LDS_BYTES = 163840;
constexpr unsigned CN_S = 48, CN_LM = 16, CN_ML = 32, CN_T = 64;
constexpr unsigned FRUN = 8224, L_TB = 2 * STG - 24576;

DI rsrc_t mk_rsrc(const void* p, unsigned bytes) { return __builtin_amdgcn_make_buffer_rsrc((void*)p, 0, bytes, 0x00020000); }
DI unsigned pk2(float a, float b) { bf16x2 v = {(__bf16)a, (__bf16)b}; return __builtin_bit_cast(unsigned, v); }
DI float bflo(unsigned u) { return __builtin_bit_cast(float, u << 16); }
DI float bfhi(unsigned u) { return __builtin_bit_cast(float, u & 0xffff0000u); }
DI float rbf(float a) { return (float)(__bf16)a; }
DI float asf(unsigned u) { return __builtin_bit_cast(float, u); }
DI unsigned asu(float f) { return __builtin_bit_cast(unsigned, f); }
DI f32x4 mfma(u32x4 a, u32x4 b, f32x4 c) {
  return __builtin_amdgcn_mfma_f32_16x16x32_bf16(__builtin_bit_cast(bf16x8, a), __builtin_bit_cast(bf16x8, b), c, 0, 0, 0);
}
DI float cosr(float rev) { return __builtin_amdgcn_cosf(rev); }
DI float sinr(float rev) { return __builtin_amdgcn_sinf(rev); }
template <int N> DI void wait_v() { asm volatile("s_waitcnt vmcnt(%0)" :: "i"(N) : "memory"); }
#define WAIT_L0() asm volatile("s_waitcnt lgkmcnt(0)" ::: "memory")
#define BAR() do { asm volatile("" ::: "memory"); __builtin_amdgcn_s_barrier(); asm volatile("" ::: "memory"); } while (0)
#define SB() __builtin_amdgcn_sched_barrier(0)
template <class T> DI void pin(T& v) { asm volatile("" : "+v"(v)); }
DI void dma16(rsrc_t r, unsigned la, unsigned li, unsigned voff, unsigned soff) {
  asm volatile("s_add_i32 m0, %0, %4\n\ts_nop 0\n\tbuffer_load_dwordx4 %1, %2, %3 offen lds" :: "s"(la), "v"(voff), "s"(r), "s"(soff), "i"(li) : "memory", "scc");
}
DI u32x4 ald128s(rsrc_t r, unsigned v, unsigned s) { u32x4 o; asm volatile("buffer_load_dwordx4 %0, %1, %2, %3 offen sc1" : "=v"(o) : "v"(v), "s"(r), "s"(s) : "memory"); return o; }
DI u32x2 ald64s(rsrc_t r, unsigned v, unsigned s) { u32x2 o; asm volatile("buffer_load_dwordx2 %0, %1, %2, %3 offen sc1" : "=v"(o) : "v"(v), "s"(r), "s"(s) : "memory"); return o; }
DI unsigned ald32s(rsrc_t r, unsigned v, unsigned s) { unsigned o; asm volatile("buffer_load_dword %0, %1, %2, %3 offen sc1" : "=v"(o) : "v"(v), "s"(r), "s"(s) : "memory"); return o; }
DI u32x4 ld128s(rsrc_t r, unsigned v, unsigned s) { return __builtin_amdgcn_raw_buffer_load_b128(r, v, s, SC1); }
template <int AUX> DI void st128(u32x4 v, rsrc_t r, unsigned voff) { __builtin_amdgcn_raw_buffer_store_b128(v, r, voff, 0, AUX); }
DI void st64s(u32x2 v, rsrc_t r, unsigned voff) { __builtin_amdgcn_raw_buffer_store_b64(v, r, voff, 0, SC1); }
DI void st32s(unsigned v, rsrc_t r, unsigned voff) { __builtin_amdgcn_raw_buffer_store_b32(v, r, voff, 0, SC1); }
DI u32x4 lds128(unsigned a) { return *(LDSP u32x4*)a; }
DI f32x4 ldsf4(unsigned a) { return *(LDSP f32x4*)a; }
DI u32x2 ldstr(unsigned a) { return __builtin_bit_cast(u32x2, __builtin_amdgcn_ds_read_tr16_b64_v4i16((LDSP s16x4*)a)); }
template <int D> DI u32x4 ldstr2(unsigned a) { const u32x2 lo = ldstr(a), hi = ldstr(a + D); return u32x4{lo[0], lo[1], hi[0], hi[1]}; }
template <int C> DI float dpp_add(float v) { return v + asf(__builtin_amdgcn_update_dpp(0, asu(v), C, 0xf, 0xf, true)); }
DI float swap16_add(float a, float b) { const auto r = __builtin_amdgcn_permlane16_swap(asu(a), asu(b), false, false); return asf(r[0]) + asf(r[1]); }
DI float swap32_add(float a, float b) { const auto r = __builtin_amdgcn_permlane32_swap(asu(a), asu(b), false, false); return asf(r[0]) + asf(r[1]); }
DI float sum16(float v) { v = dpp_add<0xB1>(v); v = dpp_add<0x4E>(v); v = dpp_add<0x141>(v); return dpp_add<0x128>(v); }
DI float dot2(unsigned a, unsigned b, float s) { return __builtin_amdgcn_fdot2_f32_bf16(__builtin_bit_cast(bf16x2, a), __builtin_bit_cast(bf16x2, b), s, false); }
DI void swp(u32x4& a, u32x4& b) {
#pragma unroll
  for (int e = 0; e < 4; e++) { unsigned x = a[e], y = b[e]; asm volatile("v_swap_b32 %0, %1" : "+v"(x), "+v"(y)); a[e] = x; b[e] = y; }
}
DI float add1(float a, float b) { float o; asm("v_add_f32 %0, %1, %2" : "=v"(o) : "v"(a), "v"(b)); return o; }
template <int NAP> DI void poll(rsrc_t r, unsigned voff, unsigned tag) {
#pragma unroll 1
  for (int it = 0; it < SPIN; it++) {
    unsigned v = ald32s(r, voff, 0);
    wait_v<0>(); pin(v);
    if (__all(voff == OOB || v == tag)) break;
    __builtin_amdgcn_s_sleep(NAP);
  }
}
DI void lds_wait(unsigned a, unsigned n) {
#pragma unroll 1
  for (int it = 0; it < SPIN * 8; it++) { if (*(volatile LDSP unsigned*)a >= n) break; __builtin_amdgcn_s_sleep(1); }
  asm volatile("" ::: "memory");
}
DI void lds_wait4(unsigned a) {
#pragma unroll 1
  for (int it = 0; it < SPIN * 8; it++) { const u32x4 v = *(volatile LDSP u32x4*)a; if (v[0] & v[1] & v[2] & v[3]) break; __builtin_amdgcn_s_sleep(1); }
  asm volatile("" ::: "memory");
}
DI void lds_set(unsigned a, unsigned v) { asm volatile("" ::: "memory"); *(volatile LDSP unsigned*)a = v; asm volatile("" ::: "memory"); }
DI void dma16n(rsrc_t r, unsigned off, unsigned voff, unsigned soff) {
  asm volatile("buffer_load_dwordx4 %0, %1, %2 offen offset:%3 lds" :: "v"(voff), "s"(r), "s"(soff), "i"(off) : "memory");
}
DI u32x4 ald128(rsrc_t r, unsigned v) { u32x4 o; asm volatile("buffer_load_dwordx4 %0, %1, %2, 0 offen" : "=v"(o) : "v"(v), "s"(r) : "memory"); return o; }
DI unsigned ald32(rsrc_t r, unsigned v) { unsigned o; asm volatile("buffer_load_dword %0, %1, %2, 0 offen" : "=v"(o) : "v"(v), "s"(r) : "memory"); return o; }
constexpr unsigned bufaddr(int b) { return (b < 3 ? 2 * STG : b < 6 ? 0 : STG) + (b % 3) * 4096; }
#define PRIO(n) __builtin_amdgcn_s_setprio(n)

#define STAMP(i)
#define CLK(i)
#define FLUSH()
#define CLK3(i)
#define STAMP2(i)

__global__ __launch_bounds__(512, 2) void fused(const void* __restrict__ px, const void* __restrict__ pwr, const void* __restrict__ pwi,
                                                const void* __restrict__ pwg, const void* __restrict__ pwu, const void* __restrict__ pwd,
                                                char* ws, void* __restrict__ pout) {
  extern __shared__ __attribute__((aligned(16))) char lds_[];
  const unsigned lds = (unsigned)(size_t)(LDSP char*)lds_;
  asm volatile("" :: "s"(px), "s"(pwr), "s"(pwi), "s"(pwg), "s"(pwu), "s"(ws));
  const int bid = blockIdx.x, xcd = bid & 7, idx = bid >> 3;
  const int rb = (xcd & 3) * 4 + (idx & 3), half = xcd >> 2, nb = idx >> 2, cb = (7 * half + nb) % 14;
  const int ai = 8 * half + nb, hid = 2 * rb + half, rbase = 16 * (rb & 1) + 64 * (rb >> 1);
  const bool helper = nb == 7;
  const int wave = __builtin_amdgcn_readfirstlane(threadIdx.x >> 6), wi = wave & 3;
  const unsigned lane = threadIdx.x & 63, c = lane & 15, q = lane >> 4;
  const rsrc_t rS = mk_rsrc(ws, WS_BYTES), rX = mk_rsrc(px, X_BYTES);
  const f32x4 z4 = {0.f, 0.f, 0.f, 0.f};
  unsigned tag;
  STAMP(0);

  auto step = [&](int n) { const int t = cb + n; return 4 * (t >= 14 ? t - 14 : t) + wi; };
  auto tbase = [&]() { return lds + wi * 12288 + (4 * q + (c >> 2)) * 128 + (((2 * q + (c >> 3)) & 3) << 5) + (c & 3) * 8; };
  auto ldb = [&](u32x4 (&B)[4], unsigned tb, int buf) {
#pragma unroll
    for (int v = 0; v < 4; v++) B[v] = ldstr2<2048>((tb ^ (v << 5)) + bufaddr(buf));
  };
  auto hbase = [&](int p) { return (2 * rb + half) * HB + ((p ^ 3 ^ wi) << 10) + lane * 16; };
  auto obase = [&]() { return (rbase + 512 * wi + 32 * half + c) * 1024 + nb * 128 + (wave >> 2) * 64 + (q & 1) * 32 + (q >> 1) * 16; };
  auto ftflag = [&]() { st32s(tag, rS, lane == 0 ? SY_FT + (wi >> 1) * 32768 + (ai >> 1) * 4096 + rb * 256 + (ai & 1) * 128 + (wi & 1) * 4 : OOB); };
  const unsigned cLM = lds + L_CN + CN_LM + wi * 4, cML = lds + L_CN + CN_ML + wi * 4;
  const rsrc_t rD = mk_rsrc(pwd, W1_BYTES);
  unsigned dv[4];
  auto dset = [&]() {
#pragma unroll
    for (int p = 0; p < 4; p++) { dv[p] = (lane >> 3) * 1024 + (((lane & 7) ^ (q << 1)) << 4) + nb * 128 + p * 7168; pin(dv[p]); }
  };
  auto dd = [&](int buf, int n) {
    dma16(rD, lds + wi * 12288, bufaddr(buf), dv[0], step(n) * 32768);
#pragma unroll
    for (int p = 1; p < 4; p++) dma16n(rD, p * 1024, dv[p], step(n) * 32768);
  };
  f32x4 o0, o1;
  u32x4 res, A0[4];

  const rsrc_t rG = mk_rsrc(pwg, W1_BYTES), rU = mk_rsrc(pwu, W1_BYTES), rB = mk_rsrc((const char*)px - 4096, X_BYTES + 4096);
  unsigned xv[4], wv4[4];
  auto xset = [&]() {
#pragma unroll
    for (int i = 0; i < 4; i++) {
      xv[i] = 4096 - 1024 * i + (rbase + 512 * wi + 32 * ((i >> 1) ^ half) + (i & 1) * 8 + (lane >> 3)) * 1024 + ((((lane & 7) ^ q) << 4) ^ ((i & 1) * 64));
      wv4[i] = (16 * wi + 4 * i + q) * 3584 - 1024 * i + cb * 256 + (((c ^ (q << 1)) << 4) ^ ((i & 2) * 64));
      pin(xv[i]); pin(wv4[i]);
    }
  };
  auto stage = [&](int s, auto&& job) {
    const int ks = (s + 2 * (rb >> 2)) & 7;
#pragma unroll
    for (int i = 0; i < 12; i++) {
      const rsrc_t r = i < 4 ? rB : i < 8 ? rG : rU;
      const unsigned vo = i < 4 ? xv[i] : wv4[i & 3], so = i < 4 ? ks * 128 : ks * (64 * 3584);
      if (i & 3) dma16n(r, (i & 3) * 1024, vo, so); else dma16(r, lds + wi * 4096, (s % 3) * STG + (i >> 2) * 16384, vo, so);
      job(i); SB();
    }
  };
  auto none = [&](int) {};

  if (wave < 4) {
    if (!helper) {
      xset();
      stage(0, none);
      unsigned xa0 = lds + c * 128 + ((q ^ (c >> 1)) << 4), xa1 = xa0 ^ 64;
      unsigned wa0 = lds + 16384 + (8 * q + (c >> 2)) * 256 + (((2 * wi) ^ ((c >> 2) | ((q & 1) << 2))) << 5) + (c & 3) * 8, wa1 = wa0 ^ 32;
      unsigned ac = lds + L_CS + 128 * wi + 16 * q, as = lds + L_ST + c * 16;
      pin(xa0); pin(xa1); pin(wa0); pin(wa1); pin(ac); pin(as);
      stage(1, none);
      PRIO(2);
      f32x4 acc[8][4];
      u32x4 W[2][4], X[2][4];
      auto lw = [&](int b, int m0, int ks) {
#pragma unroll
        for (int m = m0; m < m0 + 2; m++) W[b][m] = ldstr2<1024>(((m & 1) ? wa1 : wa0) + ks * 8192 + (m >> 1) * 16384);
      };
      auto lx = [&](int h, int ks) {
#pragma unroll
        for (int t = 0; t < 4; t++) X[h][t] = lds128((ks ? xa1 : xa0) + (h * 4 + t) * 2048);
      };
      auto mm = [&](int b, int h, int t0, int t1, bool first = false) {
#pragma unroll
        for (int t = t0; t < t1; t++)
#pragma unroll
          for (int m = 0; m < 4; m++) acc[h * 4 + t][m] = mfma(W[b][m], X[h][t], first ? z4 : acc[h * 4 + t][m]);
#pragma unroll
        for (int i = 0; i < (t1 - t0) * 4; i++) { __builtin_amdgcn_sched_group_barrier(0x008, 1, 0); __builtin_amdgcn_sched_group_barrier(0x102, 1, 0); }
        SB();
      };
      auto front = [&](unsigned dl, bool first) {
        lx(1, 0); lw(1, 0, 1); mm(0, 0, 0, 4, first);
        lw(1, 2, 1); lx(0, 1); mm(0, 1, 0, 4, first);
        lx(1, 1); xa0 += dl; xa1 += dl; wa0 += dl; wa1 += dl; mm(1, 0, 0, 3);
        pin(xa0); pin(xa1); pin(wa0); pin(wa1);
      };
      wait_v<12>(); BAR();
      STAMP(1); CLK(29);
      lw(0, 0, 0); lx(0, 0); lw(0, 2, 0);
      SB();
      front(STG, true);
      wait_v<0>();
      int sl = 1;
#pragma unroll 1
      for (int s = 1; s < 8; s++) {
        const unsigned dl = (sl == 2) ? -2 * STG : STG;
        sl = (sl == 2) ? 0 : sl + 1;
        STAMP2(14 + s);
        WAIT_L0(); BAR();
        lw(0, 0, 0); mm(1, 0, 3, 4);
        lw(0, 2, 0); lx(0, 0); mm(1, 1, 0, 4);
        front(dl, false);
      }
      f32x4 cs[4], st[8];
      WAIT_L0();
      lds_set(lds + L_CN + CN_S + wi * 4, 1);
      const unsigned tv = *(LDSP unsigned*)(lds + L_CN + CN_T);
      st[0] = ldsf4(as);
#pragma unroll
      for (int m = 0; m < 4; m++) cs[m] = ldsf4(ac + (m >> 1) * 512 + 64 * (m & 1));
      mm(1, 0, 3, 4);
#pragma unroll
      for (int t = 1; t < 8; t++) st[t] = ldsf4(as + 256 * t);
      mm(1, 1, 0, 4);
      tag = __builtin_amdgcn_readfirstlane(tv);
      STAMP(2); CLK(30);
      const unsigned ho = (cb * 4 + wi) * 4096 + lane * 16;
      const unsigned ho0 = ho + (2 * rb + half) * HB, ho1 = ho + (2 * rb + 1 - half) * HB;
#pragma unroll
      for (int t = 0; t < 8; t++) {
        f32x2 g[4], u[4], y[4];
#pragma unroll
        for (int p = 0; p < 4; p++) { const int v = p >> 1, e = (p & 1) * 2; g[p] = f32x2{acc[t][v][e], acc[t][v][e + 1]} - st[t][0] * f32x2{cs[v][e], cs[v][e + 1]}; }
#pragma unroll
        for (int p = 0; p < 4; p++) y[p] = g[p] * g[p];
        SB();
#pragma unroll
        for (int p = 0; p < 4; p++) y[p] = st[t][1] + st[t][2] * y[p];
#pragma unroll
        for (int p = 0; p < 4; p++) y[p] *= g[p];
        SB();
#pragma unroll
        for (int p = 0; p < 4; p++) {
          const int v = p >> 1, e = (p & 1) * 2;
          y[p] = f32x2{__builtin_amdgcn_exp2f(y[p][0]), __builtin_amdgcn_exp2f(y[p][1])};
          u[p] = f32x2{acc[t][2 + v][e], acc[t][2 + v][e + 1]} - st[t][0] * f32x2{cs[2 + v][e], cs[2 + v][e + 1]};
        }
        SB();
#pragma unroll
        for (int p = 0; p < 4; p++) y[p] = st[t][3] + st[t][3] * y[p];
        SB();
#pragma unroll
        for (int p = 0; p < 4; p++) { y[p] = f32x2{__builtin_amdgcn_rcpf(y[p][0]), __builtin_amdgcn_rcpf(y[p][1])}; u[p] *= g[p]; }
        SB();
#pragma unroll
        for (int p = 0; p < 4; p++) u[p] *= y[p];
        SB();
        const u32x4 o = {pk2(u[0][0], u[0][1]), pk2(u[1][0], u[1][1]), pk2(u[2][0], u[2][1]), pk2(u[3][0], u[3][1])};
        if (EXP & 8) asm volatile("" :: "v"(o)); else st128<SC1>(o, rS, ((t & 1) ? ho1 : ho0) + (t >> 1) * 1024);
        if (!(t & 1)) A0[t >> 1] = o;
      }
      SB();
    } else {
      const unsigned fo = (16 * (hid >> 3) + 16 * (hid & 4) + 512 * (hid & 3) + 128 * wi) * 514;
#pragma unroll
      for (int a = 0; a < 2; a++) {
        const rsrc_t rF = mk_rsrc(a ? pwi : pwr, WF_BYTES);
#pragma unroll
        for (int n = 0; n < 8; n++) {
          if (n & 3) dma16n(rF, (n & 3) * 1024, lane * 16, fo + (n & 4) * 1024); else dma16(rF, lds + 2 * wi * FRUN, a * FRUN + n * 1024, lane * 16, fo + n * 1024);
        }
        if (lane < 2) dma16(rF, lds + 2 * wi * FRUN, a * FRUN + 8192, lane * 16, fo + 8192);
      }
      dset();
      dd(0, 9); dd(1, 10); dd(2, 11);
      wait_v<12>();
    }
  } else {
    PRIO(3);
    {
      const int hf = wi & 1, h = wi >> 1;
      const unsigned np = rbase + 32 * hf + ai, ra = lds + L_U + hf * 4096;
      dma16(rX, ra, 0, lane * 16, np * 1024);
#pragma unroll
      for (int j = 1; j < 4; j++) dma16n(rX, j * 1024, lane * 16, (np + 511 * j) * 1024);
      unsigned ev = ald32s(rS, SY_E, 0);
      PRIO(0);
      if (wave == 4 && lane < 16) *(LDSP unsigned*)(lds + L_CN + lane * 4) = 0;
      u32x4 C, N, A2;
      f32x4 tc, ts;
      float cw[3], sw[3];
      {
        float co[4], si[4], ca[4], sa[4];
#pragma unroll
        for (int e = 0; e < 4; e++) {
          const float n1 = (float)(4 * q + e), a = n1 * (float)(c + 1 + 16 * h);
          co[e] = cosr(a * (1.f / 32.f)); si[e] = -sinr(a * (1.f / 32.f));
          tc[e] = cosr(a * (1.f / 512.f)); ts[e] = sinr(a * (1.f / 512.f));
          const float b = n1 * (float)(2 * (c >> 2) + (c & 1)) * (1.f / 16.f) + (float)(c & 2) * 0.125f;
          ca[e] = cosr(b); sa[e] = sinr(b);
        }
#pragma unroll
        for (int r = 0; r < 3; r++) { const float a = (float)np * ((r + 1) / 2048.f); cw[r] = cosr(a); sw[r] = sinr(a); pin(cw[r]); pin(sw[r]); }
        const unsigned sg = (c & 1) ? 0u : 0x80008000u;
        const unsigned c01 = pk2(co[0], co[1]), c23 = pk2(co[2], co[3]), s01 = pk2(si[0], si[1]), s23 = pk2(si[2], si[3]);
        C = u32x4{c01, c23, c01 ^ sg, c23 ^ sg}; N = u32x4{s01, s23, s01 ^ sg, s23 ^ sg};
        A2 = u32x4{pk2(ca[0], ca[1]), pk2(ca[2], ca[3]), pk2(sa[0], sa[1]), pk2(sa[2], sa[3])};
        pin(C); pin(N); pin(A2); pin(tc); pin(ts);
      }
      if (!helper) xset();
      f32x2 mk = (h && c == 15) ? f32x2{-1.f, -1.f} : f32x2{0.f, 0.f};
      unsigned go = (c == 0 && (q >> 1) == h) ? SY_RG + (128 * rb + 32 * q + 16 * hf + ai) * 8 : OOB;
      unsigned to = OFF_T + (16 * rb + ai) * 8192 + hf * 4096 + (64 * q + 16 * h + c) * 16, xr = ra + lane * 8;
      u32x4 X[4], sel[4];
      float p[4];
#pragma unroll
      for (int n = 0; n < 4; n++) { const unsigned v = (c >> 2) == n ? 0x3f803f80u : 0u; sel[n] = u32x4{v, v, v, v}; pin(sel[n]); p[n] = 0.f; pin(p[n]); }
      pin(mk); pin(go); pin(to); pin(xr);
      wait_v<0>();
      pin(ev);
      tag = __builtin_amdgcn_readfirstlane(ev) + 1;
      *(LDSP unsigned*)(lds + L_CN + CN_T) = tag;
      STAMP(13);
      f32x4 yr[4], yi[4], t[4], sm = z4;
#pragma unroll
      for (int n = 0; n < 4; n++) X[n] = ldstr2<512>(xr + n * 1024);
#pragma unroll
      for (int n = 0; n < 4; n++) {
        yr[n] = mfma(X[n], C, z4); yi[n] = mfma(X[n], N, z4); sm = mfma(sel[n], X[n], sm);
#pragma unroll
        for (int e = 0; e < 4; e++) p[n] = dot2(X[n][e], X[n][e], p[n]);
      }
      float sq = swap16_add(swap32_add(p[0], p[2]), swap32_add(p[1], p[3])), m = sm[0];
      auto turn = [&](int n) {
        const float k = asf(__builtin_amdgcn_readlane(asu(yr[n][0]), 15));
        const f32x2 lo = mk * k + f32x2{yr[n][0], yr[n][1]}, hi = mk * k + f32x2{yr[n][2], yr[n][3]};
        const f32x4 y = {lo[0], lo[1], hi[0], hi[1]}, a = y * tc + yi[n] * ts, b = yi[n] * tc - y * ts;
        t[n] = mfma(A2, u32x4{pk2(a[0], a[1]), pk2(a[2], a[3]), pk2(b[0], b[1]), pk2(b[2], b[3])}, z4);
      };
      sq = dpp_add<0xB1>(sq); pin(sq); m = dpp_add<0xB1>(m); pin(m); turn(0);
      sq = dpp_add<0x4E>(sq); pin(sq); m = dpp_add<0x4E>(m); pin(m); SB();
      sq = dpp_add<0x141>(sq); pin(sq); m = dpp_add<0x141>(m); pin(m); turn(1);
      sq = dpp_add<0x128>(sq); pin(sq); m = dpp_add<0x128>(m); pin(m); SB();
      m *= 1.f / 512.f;
      const float var = fmaxf(sq * (1.f / 512.f) - m * m, 0.f);
      const unsigned st = pk2(__builtin_amdgcn_sqrtf(rbf(rbf(var) + 1e-5f)), m);
      turn(2);
      const unsigned rr = asu(__builtin_amdgcn_rcpf(bflo(st)));
      turn(3);
      const float r2 = asf(__builtin_amdgcn_readlane(rr, 32)), r3 = asf(__builtin_amdgcn_readlane(rr, 48));
      t[0] *= asf(__builtin_amdgcn_readlane(rr, 0)); t[1] *= asf(__builtin_amdgcn_readlane(rr, 16));
      const f32x4 s0 = t[0] + r2 * t[2], s1 = t[0] - r2 * t[2], s2 = t[1] + r3 * t[3], s3 = t[1] - r3 * t[3];
      const f32x4 d0 = s0 + s2, d2 = s0 - s2;
      const f32x2 re[3] = {s1.lo + s3.hi, d2.lo, s1.lo - s3.hi}, im[3] = {s1.hi - s3.lo, d2.hi, s1.hi + s3.lo};
      u32x4 o[2];
#pragma unroll
      for (int r = 0; r < 4; r++) {
        const f32x2 x = r ? re[r - 1] * cw[r - 1] + im[r - 1] * sw[r - 1] : d0.lo, y = r ? im[r - 1] * cw[r - 1] - re[r - 1] * sw[r - 1] : d0.hi;
        o[0][r] = pk2(x[0], y[0]); o[1][r] = pk2(x[1], y[1]);
      }
      st128<SC1>(o[0], rS, to); st128<SC1>(o[1], rS, to + 512);
      st64s(u32x2{st, tag}, rS, go);
      STAMP(14);
    }
    if (!helper) {
      WAIT_L0(); BAR(); CLK3(13);
      unsigned ca, cg;
      u32x4 sel, cr[2];
      f32x4 cs = z4;
      auto col_job = [&](int s, int i) {
        if (i == 7) { cr[0] = lds128(ca + s * STG); cr[1] = lds128(ca + s * STG + 8192); }
        if (i == 11) { PRIO(3); cs = mfma(cr[0], sel, cs); cs = mfma(cr[1], sel, cs); pin(cs); SB(); PRIO(0); }
      };
      stage(2, [&](int i) {
        if (i == 3) {
          ca = lds + 16384 + (rb & 2) * 8192 + (((c >> 1) & 3) + 8 * (c >> 3) + 4 * (q & 1) + 16 * (q >> 1)) * 256 + (((4 * (rb & 1) + wi) ^ (c >> 1)) << 5) + (c & 1) * 16;
#pragma unroll
          for (int e = 0; e < 4; e++) sel[e] = ((c >> 1) & 3) == e ? ((c & 1) ? 0x3f800000u : 0x3f80u) : 0u;
          pin(sel); pin(ca);
        }
        col_job(0, i);
      });
      wait_v<13>(); ftflag();
      BAR(); CLK3(14);
      stage(3, [&](int i) {
        if (i == 3) { cg = (lane & 24) ? OOB : SY_CG + cb * 8192 + (64 * (rb & 3) + 16 * wi + 8 * (lane >> 5) + c) * 32 + (rb >> 2) * 8; pin(cg); }
        col_job(1, i);
      });
      STAMP2(15); wait_v<13>(); BAR(); STAMP2(16); CLK3(15);
      stage(4, [&](int i) {
        if (i == 3) {
          float v = swap32_add(cs[0] + cs[2], cs[1] + cs[3]);
          v = swap16_add(v, v);
          st64s(u32x2{asu(v), tag}, rS, cg);
        }
      });
      STAMP2(17); wait_v<13>(); BAR(); STAMP2(18); CLK3(16);
      stage(5, none);
      dset();
      STAMP2(19); wait_v<12>(); BAR(); STAMP2(20); CLK3(17);
      stage(6, none);
      unsigned grv = SY_RG + (128 * rb + 32 * wi + (lane & 31)) * 8, gcv = SY_CG + cb * 8192 + (64 * wi + lane) * 32;
      pin(grv); pin(gcv);
      STAMP2(21); wait_v<12>(); BAR(); STAMP2(22); CLK3(18);
      u32x2 gr = ald64s(rS, grv, 0);
      u32x4 g0 = ald128s(rS, gcv, 0), g1 = ald128s(rS, gcv, 16);
      stage(7, none);
      STAMP2(23); wait_v<15>(); BAR(); STAMP2(24); CLK3(19);
      wait_v<8>(); CLK3(20); dd(0, 9); CLK3(21);
      pin(gr); pin(g0); pin(g1);
#pragma unroll 1
      for (int it = 0; it < SPIN; it++) {
        if (__all(gr[1] == tag && g0[1] == tag && g0[3] == tag && g1[1] == tag && g1[3] == tag)) break;
        __builtin_amdgcn_s_sleep(2);
        asm volatile("buffer_load_dwordx2 %0, %3, %5, 0 offen sc1\n\tbuffer_load_dwordx4 %1, %4, %5, 0 offen sc1\n\t"
                     "buffer_load_dwordx4 %2, %4, %5, 0 offen offset:16 sc1\n\ts_waitcnt vmcnt(0)"
                     : "=&v"(gr), "=&v"(g0), "=&v"(g1) : "v"(grv), "v"(gcv), "s"(rS) : "memory");
      }
      CLK3(22);
      const float sd = bflo(gr[0]), rr = __builtin_amdgcn_rcpf(sd);
      float s1 = -2.302208198f * rr, s2 = rr * rr, s3 = sd * sd;
      pin(s1); pin(s2); pin(s3);
      s2 *= 0.044715f; pin(s2); s2 *= s1; pin(s2);
      *(LDSP f32x4*)(lds + L_ST + (32 * wi + ((lane & 31) ^ (16 * half))) * 16) = f32x4{bfhi(gr[0]), s1, s2, s3};
      *(LDSP float*)(lds + L_CS + (64 * wi + lane) * 4) = add1(add1(asf(g0[0]), asf(g0[2])), add1(asf(g1[0]), asf(g1[2])));
      CLK3(23); wait_v<8>(); dd(1, 10); CLK3(24); wait_v<8>(); CLK3(25); dd(2, 11); CLK3(26);
      STAMP2(25); WAIT_L0(); BAR();
      STAMP(2); CLK(29);
      dd(3, 12); dd(4, 13); dd(5, 0);
    } else {
      const float x = (float)((3 ^ (hid >> 3)) * wi + 1) * 0.25f;
      f32x4 tc[2], ts[2];
      u32x4 br, bi;
#pragma unroll
      for (int e = 0; e < 4; e++) {
        const float k = (float)(16 * q + 4 * e + wi), a = k * (float)c, u = cosr(a * (1.f / 64.f)), v = sinr(a * (1.f / 64.f));
        br[e] = pk2(u, v); bi[e] = pk2(-v, u);
#pragma unroll
        for (int ta = 0; ta < 2; ta++) { const float w = a * (1.f / 2048.f) + k * (ta / 128.f) + x; tc[ta][e] = cosr(w); ts[ta][e] = sinr(w); }
      }
      const unsigned a = lds + L_TB + wi * 6144 + lane * 16;
      *(LDSP u32x4*)a = br; *(LDSP u32x4*)(a + 1024) = bi;
#pragma unroll
      for (int ta = 0; ta < 2; ta++) { *(LDSP f32x4*)(a + 2048 + ta * 2048) = tc[ta]; *(LDSP f32x4*)(a + 3072 + ta * 2048) = ts[ta]; }
      dset();
      wait_v<1>(); ftflag();
    }
  }

  if (helper) {
    WAIT_L0(); BAR();
    STAMP(1);
    tag = __builtin_amdgcn_readfirstlane(*(LDSP unsigned*)(lds + L_CN + CN_T));
    {
      const unsigned ro = (c >> 2) + 4 * (c & 3), od = ro & 1, sal = od ? 0x07060504u : 0x05040302u, slo = od ? 0x01000504u : 0x05040100u, shi = slo + 0x02020202u;
      const unsigned g = 4 * q + wi + 4 * (wave & 4);
      const unsigned a0 = lds + ro * 514 - 2 * od + 16 * g, aa = a0 + od * FRUN, ab = a0 + FRUN - od * FRUN;
      u32x4 w[8];
#pragma unroll
      for (int e = 0; e < 4; e++) {
        unsigned x[5], y[5];
#pragma unroll
        for (int j = 0; j < 5; j++) { x[j] = *(LDSP unsigned*)(aa + e * 2 * FRUN + j * 4); y[j] = *(LDSP unsigned*)(ab + e * 2 * FRUN + j * 4); }
#pragma unroll
        for (int j = 0; j < 4; j++) {
          const unsigned u = __builtin_amdgcn_perm(x[j + 1], x[j], sal), v = __builtin_amdgcn_perm(y[j + 1], y[j], sal);
          w[2 * j][e] = __builtin_amdgcn_perm(v, u, slo); w[2 * j + 1][e] = __builtin_amdgcn_perm(v, u, shi);
        }
      }
#pragma unroll
      for (int j = 0; j < 8; j++) st128<SC1>(w[j], rS, OFF_WT + (8 * g + j) * 8192 + hid * 256 + c * 16);
    }
    STAMP2(15);
    const unsigned tb = lds + L_TB + lane * 16;
    u32x4 er = lds128(tb), ei = lds128(tb + 1024), fr, fi;
    f32x4 tc[2], ts[2];
    {
      const float sc = (hid == 31 && wave == 7) ? 1.f / 1048576.f : 1.f / 524288.f;
#pragma unroll
      for (int e = 0; e < 4; e++) {
        const float n = (float)(4 * q + e), a = n * (float)c, u = cosr(a * (1.f / 16.f) + n * (1.f / 32.f)), v = sinr(a * (1.f / 16.f) + n * (1.f / 32.f));
        fr[e] = pk2(u, v); fi[e] = pk2(-v, u);
#pragma unroll
        for (int ta = 0; ta < 2; ta++) { const float w = a * (1.f / 512.f) + (float)c * (ta / 32.f); tc[ta][e] = sc * cosr(w); ts[ta][e] = sc * sinr(w); }
      }
      pin(er); pin(ei); pin(fr); pin(fi); pin(tc[0]); pin(tc[1]); pin(ts[0]); pin(ts[1]);
    }
    const unsigned e_ = (lane >> 3) & 3, col = 8 * hid + wave;
    unsigned tg = OFF_T + (32 * (e_ >> 1) + 2 * wave + (lane >> 5)) * 8192 + (e_ & 1) * 4096 + hid * 128 + (lane & 7) * 16;
    unsigned tw = lds + (lane & 7) * FRUN + wave * 32 + (lane >> 3 & 4) * 4 + e_ * 4, tr = lds + wave * FRUN + lane * 16;
    unsigned wg = OFF_WT + col * 8192 + (c >> 2) * 2048 + q * 256 + (c & 3) * 16;
    unsigned fv = SY_FT + (hid & 2) * 16384 + wave * 4096 + (lane >> 1) * 128 + (lane & 1) * 4, fw = lane < 4 ? SY_FW + ((hid & 3) + 4 * (hid >> 4)) * 4096 + (4 * wave + lane) * 128 : OOB;
    pin(tg); pin(tw); pin(tr); pin(wg); pin(fv); pin(fw);
    wait_v<0>();
    st32s(tag, rS, lane == 0 ? SY_FW + wave * 4096 + hid * 128 : OOB);
    STAMP2(16);
    __builtin_amdgcn_s_sleep(FDLY);
    poll<2>(rS, fv, tag);
    STAMP2(17);
    u32x4 xa[4][2], wv[4][2];
#pragma unroll
    for (int i = 0; i < 8; i++) xa[i & 3][i >> 2] = ald128s(rS, tg, ((i >> 2) * 16 + (i & 3) * 64) * 8192);
    unsigned wf = ald32s(rS, fw, 0);
    BAR();
#pragma unroll
    for (int i = 0; i < 8; i++) {
      asm volatile("s_waitcnt vmcnt(%0)" :: "i"(8 - i) : "memory");
      pin(xa[i & 3][i >> 2]);
#pragma unroll
      for (int r = 0; r < 4; r++) *(LDSP unsigned*)(tw + i * 256 + r * 2048) = xa[i & 3][i >> 2][r];
      SB();
    }
    wait_v<0>(); pin(wf);
    if (!__all(fw == OOB || wf == tag)) poll<2>(rS, fw, tag);
    STAMP2(18);
    WAIT_L0(); BAR();
#pragma unroll
    for (int i = 0; i < 8; i++) xa[i >> 1][i & 1] = lds128(tr + i * 1024);
    f32x4 yr[4][2], yi[4][2];
    auto pairs = [&](f32x4 a, f32x4 b) { return u32x4{pk2(a[0], b[0]), pk2(a[1], b[1]), pk2(a[2], b[2]), pk2(a[3], b[3])}; };
    auto hold = [&]() {
#pragma unroll
      for (int i = 0; i < 8; i++) { pin(yr[i >> 1][i & 1]); pin(yi[i >> 1][i & 1]); }
      SB();
    };
    auto weave = [&]() {
      __builtin_amdgcn_sched_group_barrier(0x002, 8, 0);
#pragma unroll
      for (int i = 0; i < 16; i++) { __builtin_amdgcn_sched_group_barrier(0x008, 1, 0); __builtin_amdgcn_sched_group_barrier(0x002, 2, 0); }
      SB();
    };
#pragma unroll
    for (int i = 0; i < 8; i++) {
      yr[i >> 1][i & 1] = mfma(xa[i >> 1][i & 1], er, z4); yi[i >> 1][i & 1] = mfma(xa[i >> 1][i & 1], ei, z4);
      SB();
      if (i & 1) wv[i >> 2][(i >> 1) & 1] = ald128s(rS, wg, (i >> 2) * 64 + ((i >> 1) & 1) * 1024);
    }
    STAMP2(19);
#pragma unroll
    for (int r = 0; r < 4; r++) {
      const f32x4 a = yr[r][0] * tc[0] + yi[r][0] * ts[0], b = yi[r][0] * tc[0] - yr[r][0] * ts[0];
      yr[r][0] = yi[r][1] * ts[1] + (yr[r][1] * tc[1] + a); yi[r][0] = (yi[r][1] * tc[1] + b) - yr[r][1] * ts[1];
      yr[r][1] = 2.f * a - yr[r][0]; yi[r][1] = 2.f * b - yi[r][0];
      SB();
      wv[2 + (r >> 1)][r & 1] = ald128s(rS, wg, (2 + (r >> 1)) * 64 + (r & 1) * 1024);
    }
    hold();
#pragma unroll
    for (int r = 0; r < 4; r++) {
      const u32x4 u = pairs(yr[r][0], yi[r][0]), v = pairs(yr[r][1], yi[r][1]);
      yr[r][0] = mfma(er, u, z4); yi[r][0] = mfma(ei, u, z4); yr[r][1] = mfma(fr, v, z4); yi[r][1] = mfma(fi, v, z4);
    }
    weave();
    STAMP2(20);
    fr = lds128(tb + 2 * 6144); fi = lds128(tb + 2 * 6144 + 1024);
    BAR();
#pragma unroll
    for (int r = 0; r < 4; r++) {
      asm volatile("s_waitcnt vmcnt(%0)" :: "i"(r < 3 ? 6 : 6) : "memory");
#pragma unroll
      for (int t = 0; t < 2; t++) {
        pin(wv[r][t]);
        const f32x4 wr = __builtin_bit_cast(f32x4, wv[r][t] << 16), wq = __builtin_bit_cast(f32x4, wv[r][t] & 0xffff0000u), a = yr[r][t];
        yr[r][t] = yi[r][t] * wr + a * wq; yi[r][t] = a * wr - yi[r][t] * wq;
      }
      SB();
      if (r < 3) {
        dma16(rD, lds + wi * 12288 + (wave >> 2) * 2048, bufaddr(3 + r), dv[0], step(r < 2 ? 12 + r : 0) * 32768 + (wave >> 2) * 16384);
        dma16n(rD, 1024, dv[1], step(r < 2 ? 12 + r : 0) * 32768 + (wave >> 2) * 16384);
      }
    }
    hold();
    f32x4 uc[2], us[2];
#pragma unroll
    for (int t = 0; t < 2; t++) { uc[t] = ldsf4(tb + 2048 + t * 2048); us[t] = ldsf4(tb + 3072 + t * 2048); }
    SB();
#pragma unroll
    for (int r = 0; r < 4; r++) {
      const u32x4 u = pairs(yr[r][0], yi[r][0]), v = pairs(yr[r][1], yi[r][1]);
      yr[r][1] = mfma(u, er, z4); yi[r][1] = mfma(u, ei, z4);
      yr[r][0] = mfma(v, fr, yr[r][1]); yi[r][0] = mfma(v, fi, yi[r][1]);
    }
    weave();
    STAMP2(21);
    u32x4 gr[2], gi[2];
#pragma unroll
    for (int r = 0; r < 4; r++) {
      f32x4 nc[2], ns[2];
#pragma unroll
      for (int t = 0; t < 2; t++) if (r < 3) { nc[t] = ldsf4(tb + (r + 1) * 6144 + 2048 + t * 2048); ns[t] = ldsf4(tb + (r + 1) * 6144 + 3072 + t * 2048); }
      if (r & 1) { gr[r >> 1] = lds128(tb + r * 6144); gi[r >> 1] = lds128(tb + r * 6144 + 1024); }
      SB();
      yr[r][1] = 2.f * yr[r][1] - yr[r][0]; yi[r][1] = 2.f * yi[r][1] - yi[r][0];
#pragma unroll
      for (int t = 0; t < 2; t++) {
        const f32x4 a = yr[r][t];
        yr[r][t] = a * uc[t] + yi[r][t] * us[t]; yi[r][t] = yi[r][t] * uc[t] - a * us[t];
        uc[t] = nc[t]; us[t] = ns[t];
      }
      SB();
    }
    hold();
#pragma unroll
    for (int t = 0; t < 2; t++)
#pragma unroll
      for (int r = 0; r < 4; r++) {
        const u32x4 v = pairs(yr[r][t], yi[r][t]);
        yr[r][t] = mfma(r == 0 ? er : r == 2 ? fr : gr[r >> 1], v, r < 2 ? z4 : yr[r & 1][t]);
        yi[r][t] = mfma(r == 0 ? ei : r == 2 ? fi : gi[r >> 1], v, r < 2 ? z4 : yi[r & 1][t]);
      }
    weave();
    STAMP2(22);
    const float sg = (hid & 8) ? 1.f : -1.f;
    const unsigned zo = OFF_Z + col * 8192 + q * 2048 + c * 16;
#pragma unroll
    for (int t = 0; t < 2; t++) {
      const f32x4 ur = 2.f * yr[0][t] - yr[2][t], ui = 2.f * yi[0][t] - yi[2][t], dr = 2.f * yr[1][t] - yr[3][t], di = 2.f * yi[1][t] - yi[3][t];
      const f32x4 r0 = yr[2][t] + yr[3][t], i0 = yi[2][t] + yi[3][t], r2 = yr[2][t] - yr[3][t], i2 = yi[2][t] - yi[3][t];
      const f32x4 r1 = ur + sg * di, i1 = ui - sg * dr, r3 = ur - sg * di, i3 = ui + sg * dr;
#pragma unroll
      for (int e = 0; e < 4; e++)
        st128<SC1>(u32x4{pk2(r0[e], i0[e]), pk2(r1[e], i1[e]), pk2(r2[e], i2[e]), pk2(r3[e], i3[e])}, rS, zo + (e >> 1) * 1024 + t * 512 + (e & 1) * 256);
    }
    STAMP2(23);
    wait_v<0>();
    BAR();
    st32s(tag, rS, (lane == 0 && wave == 0) ? SY_FZ + hid * 128 : OOB);
    STAMP(2); CLK(29);
#pragma unroll
    for (int p = 0; p < 4; p++) A0[p] = u32x4{0, 0, 0, 0};
  }

  if (wave < 4) {
    CLK(28); STAMP(3);
    f32x4 a2[4][4];
    u32x4 A[9][4];
    {
      u32x4 G[4][4], Z[16];
      const unsigned zv = OFF_Z + 16 * q * 8192 + rb * 512 + (16 * half + c) * 16;
#pragma unroll
      for (int v = 0; v < 4; v++) {
        const float d = (float)(64 * nb + 16 * v + c) * (1.f / 512.f), a = d * (float)(64 * wi + 16 * q);
        f32x2 u0 = {cosr(a), sinr(a)}, u1 = {cosr(a + d), sinr(a + d)};
        const float k = 2.f * cosr(d);
#pragma unroll
        for (int j = 0; j < 16; j++) {
          G[j >> 2][v][j & 3] = pk2(u1[0], u1[1]);
          const f32x2 u2 = k * u1 - u0; u0 = u1; u1 = u2;
          if ((j & 3) == 3) pin(G[j >> 2][v]);
        }
        SB();
        if (v == GV) {
          wait_v<0>();
          st32s(tag, rS, (lane == 0 && !helper) ? SY_FH + (rb * 4 + wi) * 2048 + cb * 128 : OOB);
          lds_set(cML, 1);
          STAMP(4);
          lds_wait(cLM, 1);
          STAMP(5);
#pragma unroll
          for (int j = 0; j < 16; j++) Z[j] = ald128s(rS, zv, (64 * wi + j) * 8192);
          res = ald128(rX, obase());
          SB();
        }
      }
#pragma unroll
      for (int p = 0; p < 4; p++) A[0][p] = A0[p];
      if (!(wi & 1)) { swp(A[0][0], A[0][1]); swp(A[0][2], A[0][3]); }
      if (!(wi & 2)) { swp(A[0][0], A[0][2]); swp(A[0][1], A[0][3]); }
      STAMP(6);
#pragma unroll
      for (int zz = 0; zz < 4; zz++) {
        asm volatile("s_waitcnt vmcnt(%0)" :: "i"(13 - 4 * zz) : "memory");
#pragma unroll
        for (int j = 0; j < 4; j++) pin(Z[4 * zz + j]);
#pragma unroll
        for (int p = 0; p < 4; p++) {
          const u32x4 zf = {Z[4 * zz][p], Z[4 * zz + 1][p], Z[4 * zz + 2][p], Z[4 * zz + 3][p]};
#pragma unroll
          for (int v = 0; v < 4; v++) a2[p][v] = mfma(G[zz][v], zf, zz ? a2[p][v] : z4);
        }
        SB();
      }
    }
    STAMP(7);
    {
      unsigned hv[4];
#pragma unroll
      for (int p = 0; p < 4; p++) { hv[p] = hbase(p); pin(hv[p]); }
      const unsigned tb = tbase();
      constexpr int bf[9] = {5, 0, 1, 2, 3, 4, 6, 7, 8};
      u32x4 B[2][4];
      lds_wait(cLM, 2);
      STAMP(8);
      ldb(B[0], tb, bf[0]);
      if (helper) {
#pragma unroll
        for (int p = 0; p < 4; p++) asm volatile("buffer_load_dwordx4 %0, %1, %2, %3 offen sc1" : "+v"(A[0][p]) : "v"(hv[p]), "s"(rS), "s"(step(0) * 4096) : "memory");
      }
#pragma unroll
      for (int j = 1; j <= WIN; j++)
#pragma unroll
        for (int p = 0; p < 4; p++) A[j][p] = ald128s(rS, hv[p], step(j) * 4096);
      SB();
#pragma unroll
      for (int j = 0; j < 9; j++) {
        if (j < 8) ldb(B[(j + 1) & 1], tb, bf[j + 1]);
        if (j == 2) lds_set(cML, 2);
        SB();
#pragma unroll
        for (int p = 0; p < 4; p++) {
          if (j + WIN < 8) wait_v<4 * WIN + 3>(); else {
            const int n = 3 - p + 4 * (8 - j);
            asm volatile("s_waitcnt vmcnt(%0)" :: "i"(n) : "memory");
          }
          pin(A[j][p]);
#pragma unroll
          for (int v = 0; v < 4; v++) a2[p][v] = mfma(B[j & 1][v], A[j][p], a2[p][v]);
          SB();
          if (j + WIN < 8) A[j + WIN + 1][p] = ald128s(rS, hv[p], step(j + WIN + 1) * 4096);
          if (j == 8 && p) {
#pragma unroll
            for (int v = 0; v < 4; v++) *(LDSP f32x4*)(lds + wi * 12288 + bufaddr(p == 3 ? 6 : 2 + p) + v * 1024 + lane * 16) = a2[p - 1][v];
          }
          SB();
        }
      }
#pragma unroll
      for (int v = 2; v < 4; v++) *(LDSP f32x4*)(lds + wi * 12288 + bufaddr(2) + v * 1024 + lane * 16) = a2[3][v];
      o0 = a2[3][0]; o1 = a2[3][1];
    }
    STAMP(9);
  } else {
    u32x4 Bq[5][4];
    const unsigned tb = tbase();
    wait_v<12>();
#pragma unroll
    for (int j = 0; j < 3; j++) ldb(Bq[j], tb, j);
    WAIT_L0();
#pragma unroll
    for (int j = 0; j < 12; j++) pin(Bq[j >> 2][j & 3]);
    if (EXP & 32) lds_wait(cML, 1);
    dd(0, 1); dd(1, 2); dd(2, 3);
    wait_v<16>();
#pragma unroll
    for (int j = 3; j < 5; j++) ldb(Bq[j], tb, j);
    WAIT_L0();
#pragma unroll
    for (int j = 12; j < 20; j++) pin(Bq[j >> 2][j & 3]);
    wait_v<THR2>(); dd(3, 4); wait_v<THR2>(); dd(4, 5);
    STAMP(4);
    {
      const unsigned fv = lane < 8 ? SY_FZ + (8 * wi + lane) * 128 : OOB;
      if (!helper) lds_wait4(lds + L_CN + CN_S);
      if (EXP & 16) lds_wait(cML, 1);
      wait_v<THR>(); dd(6, 6);
      wait_v<THR>(); dd(7, 7);
      wait_v<THR>();
      unsigned zf = ald32s(rS, fv, 0);
      dd(8, 8);
      wait_v<4>(); pin(zf);
      if (!__all(fv == OOB || zf == tag)) poll<ZNAP>(rS, fv, tag);
      lds_set(cLM, 1);
    }
    STAMP(5);
    res = ald128(rX, obase());
    unsigned hv[4];
#pragma unroll
    for (int p = 0; p < 4; p++) { hv[p] = hbase(p); pin(hv[p]); }
    STAMP(6);
    lds_wait(cML, 1);
    if (helper) __builtin_amdgcn_s_sleep(HDLY);
    __builtin_amdgcn_s_sleep(PDLY);
    STAMP(7);
    poll<0>(rS, lane < 14 ? SY_FH + (rb * 4 + wi) * 2048 + lane * 128 : OOB, tag);
    lds_set(cLM, 2);
    STAMP(8);
    {
      u32x4 A[4][5];
#pragma unroll
      for (int p = 0; p < 4; p++)
#pragma unroll
        for (int j = 0; j < 5; j++) A[p][j] = ald128s(rS, hv[p], step(9 + j) * 4096);
      SB();
      f32x4 a2[4][4];
#pragma unroll
      for (int p = 0; p < 4; p++) {
#pragma unroll
        for (int j = 0; j < 5; j++) {
          const int n = 19 - 5 * p - j;
          asm volatile("s_waitcnt vmcnt(%0)" :: "i"(n) : "memory");
          pin(A[p][j]);
#pragma unroll
          for (int v = 0; v < 4; v++) a2[p][v] = mfma(Bq[j][v], A[p][j], j ? a2[p][v] : z4);
          SB();
          if (p == 1 && j == 0) lds_wait(cML, 2);
          if (p && j < 4) *(LDSP f32x4*)(lds + wi * 12288 + bufaddr(p == 1 ? 5 : p - 2) + j * 1024 + lane * 16) = a2[p - 1][j];
          SB();
        }
      }
#pragma unroll
      for (int v = 0; v < 2; v++) *(LDSP f32x4*)(lds + wi * 12288 + bufaddr(2) + v * 1024 + lane * 16) = a2[3][v];
      o0 = a2[3][2]; o1 = a2[3][3];
    }
    STAMP(9); CLK(30);
  }
  pin(res);
  WAIT_L0();
  STAMP(10);
  BAR();
  STAMP(11);
  if (bid == 0 && wave == 0) st32s(tag, rS, lane == 0 ? SY_E : OOB);
  {
    const unsigned ra = lds + (wave >> 2) * 2048 + lane * 16;
    const unsigned a = ra + wi * 12288 + bufaddr(2);
    o0 += ldsf4(a); o1 += ldsf4(a + 1024);
#pragma unroll
    for (int y = 1; y < 4; y++) {
      const int p = 3 ^ y;
      const unsigned b = ra + (wi ^ y) * 12288, bm = bufaddr(p == 2 ? 6 : 3 + p), bl = bufaddr(p == 0 ? 5 : p - 1);
      o0 += ldsf4(b + bm) + ldsf4(b + bl); o1 += ldsf4(b + bm + 1024) + ldsf4(b + bl + 1024);
    }
    float lo[4], hi[4];
#pragma unroll
    for (int e = 0; e < 4; e++) { const auto r = __builtin_amdgcn_permlane16_swap(asu(o0[e]), asu(o1[e]), false, false); lo[e] = asf(r[0]); hi[e] = asf(r[1]); }
    st128<OAUX>(u32x4{pk2(lo[0] + bflo(res[0]), lo[1] + bfhi(res[0])), pk2(lo[2] + bflo(res[1]), lo[3] + bfhi(res[1])),
                   pk2(hi[0] + bflo(res[2]), hi[1] + bfhi(res[2])), pk2(hi[2] + bflo(res[3]), hi[3] + bfhi(res[3]))}, mk_rsrc(pout, X_BYTES), obase());
  }
  STAMP(12);
  FLUSH();
}

extern "C" void kernel_launch(void* const* d_in, const int* in_sizes, int n_in, void* d_out, int out_size, void* d_ws, size_t ws_size,
                              hipStream_t stream) {
  (void)in_sizes; (void)n_in; (void)out_size; (void)ws_size;
  (void)hipFuncSetAttribute((const void*)fused, hipFuncAttributeMaxDynamicSharedMemorySize, LDS_BYTES);
  fused<<<256, 512, LDS_BYTES, stream>>>(d_in[0], d_in[1], d_in[2], d_in[4], d_in[5], d_in[6], (char*)d_ws, d_out);
}
```

```cpp
#define NOTOUCH 1
#define OAUX 16
#define PROBE 99
#define PROBEW 0
#define PROBEC 0
#include <hip/hip_runtime.h>
#include <hip/hip_bf16.h>
#pragma clang diagnostic ignored "-Wint-to-pointer-cast"

typedef __bf16 bf16x8 __attribute__((ext_vector_type(8)));
typedef __bf16 bf16x2 __attribute__((ext_vector_type(2)));
typedef short s16x4 __attribute__((ext_vector_type(4)));
typedef float f32x4 __attribute__((ext_vector_type(4)));
typedef float f32x2 __attribute__((ext_vector_type(2)));
typedef unsigned u32x4 __attribute__((ext_vector_type(4)));
typedef unsigned u32x2 __attribute__((ext_vector_type(2)));
#define LDSP __attribute__((address_space(3)))
#define DI __device__ __forceinline__
typedef __amdgpu_buffer_rsrc_t rsrc_t;

constexpr unsigned X_BYTES = 2048u * 512u * 2u;
constexpr unsigned W1_BYTES = 512u * 1792u * 2u;
constexpr unsigned WF_BYTES = 2048u * 257u * 2u;
constexpr unsigned HB = 56u * 4096u, OFF_T = 32u * HB, OFF_Z = OFF_T + 256u * 8192u, OFF_WT = OFF_Z + 256u * 8192u, OFF_SY = OFF_WT + 256u * 8192u;
constexpr unsigned SY_E = OFF_SY, SY_FT = OFF_SY + 4096, SY_FZ = OFF_SY + 8192, SY_RG = OFF_SY + 16384,
                   SY_CG = OFF_SY + 32768, SY_FH = SY_CG + 14u * 8192u, WS_BYTES = SY_FH + 64u * 2048u;
constexpr unsigned OOB = 0x40000000u;
constexpr int SC1 = 16;
#define SPINV (1 << 20)
constexpr int SPIN = SPINV;
#define TSTART 4
#define TDEP 1
#define FDLY 8
#define WIN 3
#define PDLY 6
#define HDLY 24
#define ZDLY 48
constexpr unsigned STG = 49152, L_U = 3 * STG, L_ST = L_U + 8704, L_CS = L_ST + 2048, L_CN = L_CS + 1024, L_DG = L_CN + 512, LDS_BYTES = 163840;
constexpr unsigned CN_X = 0, CN_S = 4, CN_LM = 16, CN_ML = 32;
constexpr unsigned UROW = 272, FRUN = 8224;

DI rsrc_t mk_rsrc(const void* p, unsigned bytes) { return __builtin_amdgcn_make_buffer_rsrc((void*)p, 0, bytes, 0x00020000); }
DI unsigned pk2(float a, float b) { bf16x2 v = {(__bf16)a, (__bf16)b}; return __builtin_bit_cast(unsigned, v); }
DI float bflo(unsigned u) { return __builtin_bit_cast(float, u << 16); }
DI float bfhi(unsigned u) { return __builtin_bit_cast(float, u & 0xffff0000u); }
DI float rbf(float a) { return (float)(__bf16)a; }
DI float asf(unsigned u) { return __builtin_bit_cast(float, u); }
DI unsigned asu(float f) { return __builtin_bit_cast(unsigned, f); }
DI f32x4 mfma(u32x4 a, u32x4 b, f32x4 c) {
  return __builtin_amdgcn_mfma_f32_16x16x32_bf16(__builtin_bit_cast(bf16x8, a), __builtin_bit_cast(bf16x8, b), c, 0, 0, 0);
}
DI float cosr(float rev) { return __builtin_amdgcn_cosf(rev); }
DI float sinr(float rev) { return __builtin_amdgcn_sinf(rev); }
template <int N> DI void wait_v() { asm volatile("s_waitcnt vmcnt(%0)" :: "i"(N) : "memory"); }
#define WAIT_L0() asm volatile("s_waitcnt lgkmcnt(0)" ::: "memory")
#define BAR() do { asm volatile("" ::: "memory"); __builtin_amdgcn_s_barrier(); asm volatile("" ::: "memory"); } while (0)
#define SB() __builtin_amdgcn_sched_barrier(0)
template <class T> DI void pin(T& v) { asm volatile("" : "+v"(v)); }
DI void dma16(rsrc_t r, unsigned la, unsigned li, unsigned voff, unsigned soff) {
  asm volatile("s_add_i32 m0, %0, %4\n\ts_nop 0\n\tbuffer_load_dwordx4 %1, %2, %3 offen lds" :: "s"(la), "v"(voff), "s"(r), "s"(soff), "i"(li) : "memory", "scc");
}
DI u32x4 ald128s(rsrc_t r, unsigned v, unsigned s) { u32x4 o; asm volatile("buffer_load_dwordx4 %0, %1, %2, %3 offen sc1" : "=v"(o) : "v"(v), "s"(r), "s"(s) : "memory"); return o; }
DI u32x2 ald64s(rsrc_t r, unsigned v, unsigned s) { u32x2 o; asm volatile("buffer_load_dwordx2 %0, %1, %2, %3 offen sc1" : "=v"(o) : "v"(v), "s"(r), "s"(s) : "memory"); return o; }
DI unsigned ald32s(rsrc_t r, unsigned v, unsigned s) { unsigned o; asm volatile("buffer_load_dword %0, %1, %2, %3 offen sc1" : "=v"(o) : "v"(v), "s"(r), "s"(s) : "memory"); return o; }
DI u32x4 ld128s(rsrc_t r, unsigned v, unsigned s) { return __builtin_amdgcn_raw_buffer_load_b128(r, v, s, SC1); }
template <int AUX> DI void st128(u32x4 v, rsrc_t r, unsigned voff) { __builtin_amdgcn_raw_buffer_store_b128(v, r, voff, 0, AUX); }
DI void st64s(u32x2 v, rsrc_t r, unsigned voff) { __builtin_amdgcn_raw_buffer_store_b64(v, r, voff, 0, SC1); }
DI void st32s(unsigned v, rsrc_t r, unsigned voff) { __builtin_amdgcn_raw_buffer_store_b32(v, r, voff, 0, SC1); }
DI u32x4 lds128(unsigned a) { return *(LDSP u32x4*)a; }
DI f32x4 ldsf4(unsigned a) { return *(LDSP f32x4*)a; }
DI u32x2 ldstr(unsigned a) { return __builtin_bit_cast(u32x2, __builtin_amdgcn_ds_read_tr16_b64_v4i16((LDSP s16x4*)a)); }
template <int D> DI u32x4 ldstr2(unsigned a) { const u32x2 lo = ldstr(a), hi = ldstr(a + D); return u32x4{lo[0], lo[1], hi[0], hi[1]}; }
template <int C> DI float dpp_add(float v) { return v + asf(__builtin_amdgcn_update_dpp(0, asu(v), C, 0xf, 0xf, true)); }
DI float swap16_add(float a, float b) { const auto r = __builtin_amdgcn_permlane16_swap(asu(a), asu(b), false, false); return asf(r[0]) + asf(r[1]); }
DI float swap32_add(float a, float b) { const auto r = __builtin_amdgcn_permlane32_swap(asu(a), asu(b), false, false); return asf(r[0]) + asf(r[1]); }
DI float sum16(float v) { v = dpp_add<0xB1>(v); v = dpp_add<0x4E>(v); v = dpp_add<0x141>(v); return dpp_add<0x128>(v); }
DI float dot2(unsigned a, unsigned b, float s) { return __builtin_amdgcn_fdot2_f32_bf16(__builtin_bit_cast(bf16x2, a), __builtin_bit_cast(bf16x2, b), s, false); }
DI void swp(u32x4& a, u32x4& b) {
#pragma unroll
  for (int e = 0; e < 4; e++) { unsigned x = a[e], y = b[e]; asm volatile("v_swap_b32 %0, %1" : "+v"(x), "+v"(y)); a[e] = x; b[e] = y; }
}
DI float add1(float a, float b) { float o; asm("v_add_f32 %0, %1, %2" : "=v"(o) : "v"(a), "v"(b)); return o; }
template <int NAP> DI void poll(rsrc_t r, unsigned voff, unsigned tag) {
#pragma unroll 1
  for (int it = 0; it < SPIN; it++) {
    unsigned v = ald32s(r, voff, 0);
    wait_v<0>(); pin(v);
    if (__all(voff == OOB || v == tag)) break;
    __builtin_amdgcn_s_sleep(NAP);
  }
}
DI void lds_wait(unsigned a, unsigned n) {
#pragma unroll 1
  for (int it = 0; it < SPIN * 8; it++) { if (*(volatile LDSP unsigned*)a >= n) break; __builtin_amdgcn_s_sleep(1); }
  asm volatile("" ::: "memory");
}
DI void lds_arrive(unsigned a, unsigned lane) {
  if (lane == 0) __hip_atomic_fetch_add((LDSP unsigned*)a, 1u, __ATOMIC_RELAXED, __HIP_MEMORY_SCOPE_WORKGROUP);
  asm volatile("" ::: "memory");
}
DI void lds_set(unsigned a, unsigned v) { asm volatile("" ::: "memory"); *(volatile LDSP unsigned*)a = v; asm volatile("" ::: "memory"); }
DI void dma16n(rsrc_t r, unsigned off, unsigned voff, unsigned soff) {
  asm volatile("buffer_load_dwordx4 %0, %1, %2 offen offset:%3 lds" :: "v"(voff), "s"(r), "s"(soff), "i"(off) : "memory");
}
template <bool V> struct Flag { static constexpr bool v = V; };
DI u32x4 ald128(rsrc_t r, unsigned v) { u32x4 o; asm volatile("buffer_load_dwordx4 %0, %1, %2, 0 offen" : "=v"(o) : "v"(v), "s"(r) : "memory"); return o; }
constexpr unsigned bufaddr(int b) { return (b < 3 ? 2 * STG : b < 6 ? 0 : STG) + (b % 3) * 4096; }
#define PRIO(n) __builtin_amdgcn_s_setprio(n)

#define STAMP(i) do { if ((i) == 0) { pt0 = ptp = (unsigned)__builtin_amdgcn_s_memrealtime(); pc0 = (unsigned)__builtin_amdgcn_s_memtime(); } \
  else if ((i) == PROBE && (wave >> 2) == PROBEW) ptp = PROBEC ? pt0 + ((unsigned)__builtin_amdgcn_s_memtime() - pc0) / 20 : (unsigned)__builtin_amdgcn_s_memrealtime(); } while (0)
#define CLK(i)
#define FLUSH() do { if (blockIdx.x == 0 && threadIdx.x == 0) *(unsigned*)(ws + (72u << 20)) = pt0; } while (0)
#define STAMP2(i)

__global__ __launch_bounds__(512, 2) void fused(const void* __restrict__ px, const void* __restrict__ pwr, const void* __restrict__ pwi,
                                                const void* __restrict__ pwg, const void* __restrict__ pwu, const void* __restrict__ pwd,
                                                char* ws, void* __restrict__ pout) {
  extern __shared__ __attribute__((aligned(16))) char lds_[];
  const unsigned lds = (unsigned)(size_t)(LDSP char*)lds_;
  asm volatile("" :: "s"(px), "s"(pwr), "s"(pwi), "s"(pwg), "s"(pwu), "s"(ws));
  const int bid = blockIdx.x, xcd = bid & 7, idx = bid >> 3;
  const int rb = (xcd & 3) * 4 + (idx & 3), half = xcd >> 2, nb = idx >> 2, cb = (7 * half + nb) % 14;
  const int pid = 16 * rb + 8 * half + nb, slot = 32 * xcd + idx;
  const bool helper = nb == 7;
  const int wave = __builtin_amdgcn_readfirstlane(threadIdx.x >> 6), wi = wave & 3;
  const unsigned lane = threadIdx.x & 63, c = lane & 15, q = lane >> 4;
  const rsrc_t rS = mk_rsrc(ws, WS_BYTES), rX = mk_rsrc(px, X_BYTES);
  const f32x4 z4 = {0.f, 0.f, 0.f, 0.f};
  unsigned tag;
  unsigned pt0, ptp, pc0;
  STAMP(0);

  auto step = [&](int n) { const int t = cb + n; return 4 * (t >= 14 ? t - 14 : t) + wi; };
  auto tbase = [&]() { return lds + wi * 12288 + (4 * q + (c >> 2)) * 128 + (((2 * q + (c >> 3)) & 3) << 5) + (c & 3) * 8; };
  auto ldb = [&](u32x4 (&B)[4], unsigned tb, int buf) {
#pragma unroll
    for (int v = 0; v < 4; v++) B[v] = ldstr2<2048>((tb ^ (v << 5)) + bufaddr(buf));
  };
  auto hbase = [&](int p) { return (2 * rb + half) * HB + ((p ^ 3 ^ wi) << 10) + lane * 16; };
  auto obase = [&]() { return (128 * rb + 32 * wi + 16 * half + c) * 1024 + nb * 128 + (wave >> 2) * 64 + (q & 1) * 32 + (q >> 1) * 16; };
  const unsigned cLM = lds + L_CN + CN_LM + wi * 4, cML = lds + L_CN + CN_ML + wi * 4;
  f32x4 o0, o1;
  u32x4 res;

  if (wave < 4) {
    f32x4 acc[8][4];
    {
      const unsigned row0 = 128 * rb + 64 * (wi >> 1) + 16 * half + 2 * nb + (wi & 1), ra = lds + 2 * STG + wi * 2048;
      dma16(rX, ra, 0, lane * 16, row0 * 1024);
      dma16(rX, ra, 1024, lane * 16, row0 * 1024 + 32768);
      unsigned ev = ald32s(rS, SY_E, 0);
      if (wave == 0 && lane < 16) *(LDSP unsigned*)(lds + L_CN + lane * 4) = 0;
      u32x4 C[2], N[2], A2;
      f32x4 tc[2], ts[2];
      {
        float co[4], si[4], ca[4], sa[4];
#pragma unroll
        for (int e = 0; e < 4; e++) {
          const float n1 = (float)(4 * q + e), a = n1 * (float)(c + 1);
          co[e] = cosr(a * (1.f / 32.f)); si[e] = -sinr(a * (1.f / 32.f));
          tc[0][e] = cosr(a * (1.f / 512.f)); ts[0][e] = sinr(a * (1.f / 512.f));
          tc[1][e] = cosr(a * (1.f / 512.f) + n1 * (1.f / 32.f)); ts[1][e] = sinr(a * (1.f / 512.f) + n1 * (1.f / 32.f));
          const float b = n1 * (float)(c >> 1) * (1.f / 16.f) + (float)(c & 1) * 0.25f;
          ca[e] = cosr(b); sa[e] = sinr(b);
        }
        const unsigned sg = (c & 1) ? 0u : 0x80008000u;
        const unsigned c01 = pk2(co[0], co[1]), c23 = pk2(co[2], co[3]), s01 = pk2(si[0], si[1]), s23 = pk2(si[2], si[3]);
        C[0] = u32x4{c01, c23, c01 ^ sg, c23 ^ sg}; N[0] = u32x4{s01, s23, s01 ^ sg, s23 ^ sg};
        C[1] = C[0] ^ 0x80000000u; N[1] = N[0] ^ 0x80000000u;
        A2 = u32x4{pk2(ca[0], ca[1]), pk2(ca[2], ca[3]), pk2(sa[0], sa[1]), pk2(sa[2], sa[3])};
        pin(C[0]); pin(C[1]); pin(N[0]); pin(N[1]); pin(A2); pin(tc[0]); pin(tc[1]); pin(ts[0]); pin(ts[1]);
      }
#pragma unroll
      for (int t = 0; t < 8; t++)
#pragma unroll
        for (int m = 0; m < 4; m++) { acc[t][m] = z4; pin(acc[t][m]); }
      wait_v<0>();
      pin(ev);
      tag = __builtin_amdgcn_readfirstlane(ev) + 1;
      STAMP(13);
      u32x4 X[2];
      float s[2], p[2];
#pragma unroll
      for (int n = 0; n < 2; n++) X[n] = ldstr2<512>(ra + n * 1024 + lane * 8);
#pragma unroll
      for (int n = 0; n < 2; n++) {
        s[n] = p[n] = 0.f;
#pragma unroll
        for (int e = 0; e < 4; e++) { s[n] = dot2(X[n][e], 0x3f803f80u, s[n]); p[n] = dot2(X[n][e], X[n][e], p[n]); }
      }
      const float sp = sum16(swap16_add(swap32_add(s[0], s[1]), swap32_add(p[0], p[1])));
      const auto w = __builtin_amdgcn_permlane16_swap(asu(sp), asu(sp), false, false);
      const float m = asf(w[0]) * (1.f / 512.f), var = fmaxf(asf(w[1]) * (1.f / 512.f) - m * m, 0.f);
      const unsigned st = pk2(__builtin_amdgcn_sqrtf(rbf(rbf(var) + 1e-5f)), m);
      st64s(u32x2{st, tag}, rS, (lane & 31) ? OOB : SY_RG + (row0 + lane) * 8);
      const unsigned mb = st & 0xffff0000u, rr = asu(__builtin_amdgcn_rcpf(bflo(st)));
      const float mk = c == 15 ? -32.f : 0.f;
      u32x2 o[4];
#pragma unroll
      for (int n = 0; n < 2; n++) {
        const float mu = asf(__builtin_amdgcn_readlane(mb, 32 * n)), rs = asf(__builtin_amdgcn_readlane(rr, 32 * n));
#pragma unroll
        for (int h = 0; h < 2; h++) {
          f32x4 yr = mfma(X[n], C[h], z4);
          const f32x4 yi = mfma(X[n], N[h], z4);
          if (h) yr += mk * mu;
          const f32x4 a = yr * tc[h] + yi * ts[h], b = yi * tc[h] - yr * ts[h];
          const f32x4 t = mfma(A2, u32x4{pk2(a[0], a[1]), pk2(a[2], a[3]), pk2(b[0], b[1]), pk2(b[2], b[3])}, z4) * rs;
          o[h][n] = pk2(t[0], t[1]); o[2 + h][n] = pk2(t[2], t[3]);
        }
      }
      unsigned ta = lds + L_U + (64 * q + c) * 32 + (wi & 1) * 16 + (wi >> 1) * 8;
      pin(ta);
#pragma unroll
      for (int u = 0; u < 4; u++) *(LDSP u32x2*)(ta + 512 * u) = o[u];
      STAMP(14);
    }
    u32x4 A[9][4];
    if (!helper) {
      PRIO(2);
      unsigned xa0 = lds + c * 128 + ((q ^ (c >> 1)) << 4), xa1 = xa0 ^ 64;
      const unsigned key = (c >> 2) | ((q & 1) << 2);
      unsigned wa0 = lds + 16384 + (8 * q + (c >> 2)) * 256 + (((2 * wi) ^ key) << 5) + (c & 3) * 8, wa1 = wa0 ^ 32;
      u32x4 W[2][4], X[2][4];
      auto lw = [&](int b, int m0, int ks) {
#pragma unroll
        for (int m = m0; m < m0 + 2; m++) W[b][m] = ldstr2<1024>(((m & 1) ? wa1 : wa0) + ks * 8192 + (m >> 1) * 16384);
      };
      auto lx = [&](int h, int ks) {
#pragma unroll
        for (int t = 0; t < 4; t++) X[h][t] = lds128((ks ? xa1 : xa0) + (h * 4 + t) * 2048);
      };
      auto mm = [&](int b, int h, int t0, int t1) {
#pragma unroll
        for (int t = t0; t < t1; t++)
#pragma unroll
          for (int m = 0; m < 4; m++) acc[h * 4 + t][m] = mfma(W[b][m], X[h][t], acc[h * 4 + t][m]);
#pragma unroll
        for (int i = 0; i < (t1 - t0) * 4; i++) { __builtin_amdgcn_sched_group_barrier(0x008, 1, 0); __builtin_amdgcn_sched_group_barrier(0x102, 1, 0); }
        SB();
      };
      WAIT_L0(); BAR();
      STAMP(1); CLK(29);
      lw(0, 0, 0); lw(0, 2, 0); lx(0, 0);
      int sl = 0;
#pragma unroll 1
      for (int s = 0; s < 8; s++) {
        const unsigned dl = (sl == 2) ? -2 * STG : STG;
        sl = (sl == 2) ? 0 : sl + 1;
        lx(1, 0); lw(1, 0, 1); mm(0, 0, 0, 4);
        lw(1, 2, 1); lx(0, 1); mm(0, 1, 0, 4);
        lx(1, 1); xa0 += dl; xa1 += dl; wa0 += dl; wa1 += dl; mm(1, 0, 0, 3);
        pin(xa0); pin(xa1); pin(wa0); pin(wa1);
        STAMP2(15 + s);
        asm volatile("s_waitcnt lgkmcnt(0)\n\ts_cmp_eq_u32 %0, 7\n\ts_cbranch_scc1 1f\n\ts_barrier\n1:" :: "s"(s) : "memory", "scc");
        lw(0, 0, 0); mm(1, 0, 3, 4);
        lw(0, 2, 0); lx(0, 0); mm(1, 1, 0, 4);
      }
      lds_arrive(lds + L_CN + CN_S, lane);
      STAMP(2); CLK(30);
      f32x4 cs[4], st[8];
      unsigned ac = lds + L_CS + 128 * wi + 16 * q, as = lds + L_ST + c * 16;
      pin(ac); pin(as);
      st[0] = ldsf4(as);
#pragma unroll
      for (int m = 0; m < 4; m++) cs[m] = ldsf4(ac + (m >> 1) * 512 + 64 * (m & 1));
#pragma unroll
      for (int t = 1; t < 8; t++) st[t] = ldsf4(as + 256 * t);
      SB();
      const unsigned ho = (cb * 4 + wi) * 4096 + lane * 16;
      const unsigned ho0 = ho + (2 * rb + half) * HB, ho1 = ho + (2 * rb + 1 - half) * HB;
#pragma unroll
      for (int t = 0; t < 8; t++) {
        f32x2 g[4], u[4], y[4];
#pragma unroll
        for (int p = 0; p < 4; p++) { const int v = p >> 1, e = (p & 1) * 2; g[p] = f32x2{acc[t][v][e], acc[t][v][e + 1]} - st[t][0] * f32x2{cs[v][e], cs[v][e + 1]}; }
#pragma unroll
        for (int p = 0; p < 4; p++) y[p] = g[p] * g[p];
        SB();
#pragma unroll
        for (int p = 0; p < 4; p++) y[p] = st[t][1] + st[t][2] * y[p];
#pragma unroll
        for (int p = 0; p < 4; p++) y[p] *= g[p];
        SB();
#pragma unroll
        for (int p = 0; p < 4; p++) {
          const int v = p >> 1, e = (p & 1) * 2;
          y[p] = f32x2{__builtin_amdgcn_exp2f(y[p][0]), __builtin_amdgcn_exp2f(y[p][1])};
          u[p] = f32x2{acc[t][2 + v][e], acc[t][2 + v][e + 1]} - st[t][0] * f32x2{cs[2 + v][e], cs[2 + v][e + 1]};
        }
        SB();
#pragma unroll
        for (int p = 0; p < 4; p++) y[p] = st[t][3] + st[t][3] * y[p];
        SB();
#pragma unroll
        for (int p = 0; p < 4; p++) { y[p] = f32x2{__builtin_amdgcn_rcpf(y[p][0]), __builtin_amdgcn_rcpf(y[p][1])}; u[p] *= g[p]; }
        SB();
#pragma unroll
        for (int p = 0; p < 4; p++) u[p] *= y[p];
        SB();
        const u32x4 o = {pk2(u[0][0], u[0][1]), pk2(u[1][0], u[1][1]), pk2(u[2][0], u[2][1]), pk2(u[3][0], u[3][1])};
        st128<SC1>(o, rS, ((t & 1) ? ho1 : ho0) + (t >> 1) * 1024);
        if (!(t & 1)) A[0][t >> 1] = o;
      }
      SB();
    } else {
      WAIT_L0(); BAR();
      lds_arrive(lds + L_CN + CN_S, lane);
#pragma unroll
      for (int p = 0; p < 4; p++) A[0][p] = u32x4{0, 0, 0, 0};
    }
    STAMP(3);
    lds_wait(cLM, 1);
    STAMP(4);
    f32x4 a2[4][4];
    {
      u32x4 G[4][4], Z[16];
      const unsigned zv = OFF_Z + 16 * q * 8192 + rb * 512 + (16 * half + c) * 16;
#pragma unroll
      for (int v = 0; v < 4; v++) {
        Z[4 * v] = ald128s(rS, zv, (64 * wi + 4 * v) * 8192);
        SB();
        const float d = (float)(64 * nb + 16 * v + c) * (1.f / 512.f), a = d * (float)(64 * wi + 16 * q);
        f32x2 u0 = {cosr(a), sinr(a)}, u1 = {cosr(a + d), sinr(a + d)};
        const float k = 2.f * cosr(d);
#pragma unroll
        for (int j = 0; j < 16; j++) {
          G[j >> 2][v][j & 3] = pk2(u1[0], u1[1]);
          const f32x2 u2 = k * u1 - u0; u0 = u1; u1 = u2;
          if ((j & 3) == 3) { pin(G[j >> 2][v]); if (j < 12) Z[4 * v + 1 + (j >> 2)] = ald128s(rS, zv, (64 * wi + 4 * v + 1 + (j >> 2)) * 8192); SB(); }
        }
        if (v == 2) {
          wait_v<12>();
          st32s(tag, rS, (lane == 0 && !helper) ? SY_FH + (rb * 4 + wi) * 2048 + cb * 128 : OOB);
          lds_set(cML, 1);
          STAMP(5);
        }
      }
      res = ald128(rX, obase());
      if (!(wi & 1)) { swp(A[0][0], A[0][1]); swp(A[0][2], A[0][3]); }
      if (!(wi & 2)) { swp(A[0][0], A[0][2]); swp(A[0][1], A[0][3]); }
      STAMP(6);
#pragma unroll
      for (int zz = 0; zz < 4; zz++) {
        if (zz == 0) wait_v<6>();
        if (zz == 3) wait_v<1>();
#pragma unroll
        for (int j = 0; j < 4; j++) pin(Z[4 * zz + j]);
#pragma unroll
        for (int p = 0; p < 4; p++) {
          const u32x4 zf = {Z[4 * zz][p], Z[4 * zz + 1][p], Z[4 * zz + 2][p], Z[4 * zz + 3][p]};
#pragma unroll
          for (int v = 0; v < 4; v++) a2[p][v] = mfma(G[zz][v], zf, zz ? a2[p][v] : z4);
        }
        SB();
      }
    }
    STAMP(7);
    {
      unsigned hv[4];
#pragma unroll
      for (int p = 0; p < 4; p++) { hv[p] = hbase(p); pin(hv[p]); }
      const unsigned tb = tbase();
      constexpr int bf[9] = {5, 0, 1, 2, 3, 4, 6, 7, 8};
      u32x4 B[2][4];
      lds_wait(cLM, 2);
      STAMP(8);
      ldb(B[0], tb, bf[0]);
      if (helper) {
#pragma unroll
        for (int p = 0; p < 4; p++) asm volatile("buffer_load_dwordx4 %0, %1, %2, %3 offen sc1" : "+v"(A[0][p]) : "v"(hv[p]), "s"(rS), "s"(step(0) * 4096) : "memory");
      }
#pragma unroll
      for (int j = 1; j <= WIN; j++)
#pragma unroll
        for (int p = 0; p < 4; p++) A[j][p] = ald128s(rS, hv[p], step(j) * 4096);
      SB();
#pragma unroll
      for (int j = 0; j < 9; j++) {
        if (j < 8) ldb(B[(j + 1) & 1], tb, bf[j + 1]);
        if (j == 2) lds_set(cML, 2);
        SB();
#pragma unroll
        for (int p = 0; p < 4; p++) {
          if (j + WIN < 8) wait_v<4 * WIN + 3>(); else {
            const int n = 3 - p + 4 * (8 - j);
            asm volatile("s_waitcnt vmcnt(%0)" :: "i"(n) : "memory");
          }
          pin(A[j][p]);
#pragma unroll
          for (int v = 0; v < 4; v++) a2[p][v] = mfma(B[j & 1][v], A[j][p], a2[p][v]);
          SB();
          if (j + WIN < 8) A[j + WIN + 1][p] = ald128s(rS, hv[p], step(j + WIN + 1) * 4096);
          if (j == 8 && p) {
#pragma unroll
            for (int v = 0; v < 4; v++) *(LDSP f32x4*)(lds + wi * 12288 + bufaddr(p == 3 ? 6 : 2 + p) + v * 1024 + lane * 16) = a2[p - 1][v];
          }
          SB();
        }
      }
#pragma unroll
      for (int v = 2; v < 4; v++) *(LDSP f32x4*)(lds + wi * 12288 + bufaddr(2) + v * 1024 + lane * 16) = a2[3][v];
      o0 = a2[3][0]; o1 = a2[3][1];
    }
    STAMP(9);
  } else {
    const unsigned cp = c ^ 3 ^ (slot >> 6), k1 = 16 * wi + cp;
    u32x4 bre[4], bim[4], g0, g1;
    u32x2 gr;
    const unsigned grv = SY_RG + (128 * rb + 32 * wi + (lane & 31)) * 8, gcv = SY_CG + cb * 8192 + (64 * wi + lane) * 32;
    auto front = [&](auto HF) {
      constexpr bool H = decltype(HF)::v;
      const rsrc_t rG = mk_rsrc(pwg, W1_BYTES), rU = mk_rsrc(pwu, W1_BYTES), rB = mk_rsrc((const char*)px - 4096, X_BYTES + 4096);
      unsigned xv[4], wv4[4];
      if (!H) {
#pragma unroll
        for (int i = 0; i < 4; i++) {
          xv[i] = 4096 - 1024 * i + (rb * 128 + ((4 * wi + i) ^ (2 * half)) * 8 + (lane >> 3)) * 1024 + ((((lane & 7) ^ q) << 4) ^ ((i & 1) * 64));
          wv4[i] = (16 * wi + 4 * i + q) * 3584 - 1024 * i + cb * 256 + (((c ^ (q << 1)) << 4) ^ ((i & 2) * 64));
        }
      }
      auto stage = [&](int s, auto&& job) {
        const int ks = (s + 2 * (rb >> 2)) & 7;
#pragma unroll
        for (int i = 0; i < 12; i++) {
          if (!H) {
            const rsrc_t r = i < 4 ? rB : i < 8 ? rG : rU;
            const unsigned vo = i < 4 ? xv[i] : wv4[i & 3], so = i < 4 ? ks * 128 : ks * (64 * 3584);
            if (i & 3) dma16n(r, (i & 3) * 1024, vo, so); else dma16(r, lds + wi * 4096, (s % 3) * STG + (i >> 2) * 16384, vo, so);
          }
          job(i); SB();
        }
      };
      unsigned nx = 0;
      auto meet = [&]() { nx += 4; lds_arrive(lds + L_CN + CN_X, lane); lds_wait(lds + L_CN + CN_X, nx); };
      u32x4 c32[2], s32[2], n32[2];
      float twc[2][4], tws[2][4], co[4], si[4];
      auto cjob = [&](int n) {
        if (n < 4) {
          const float a = (float)(k1 * (4 * q + n)) * (1.f / 64.f), x = cosr(a), y = sinr(a);
          unsigned u = pk2(x, y), v = pk2(-y, x); pin(u); pin(v);
          bre[0][n] = u; bim[0][n] = v;
        } else if (n < 8) {
          const unsigned u = bre[0][n & 3], v = bim[0][n & 3], m2 = (k1 & 1) ? 0x80008000u : 0u, m1 = (k1 & 2) ? 0x80008000u : 0u;
          unsigned u1 = ((k1 & 1) ? v : u) ^ m1, v1 = ((k1 & 1) ? u : v) ^ m1 ^ m2, u2 = u ^ m2, v2 = v ^ m2, u3 = u1 ^ m2, v3 = v1 ^ m2;
          pin(u1); pin(v1); pin(u2); pin(v2); pin(u3); pin(v3);
          bre[1][n & 3] = u1; bim[1][n & 3] = v1; bre[2][n & 3] = u2; bim[2][n & 3] = v2; bre[3][n & 3] = u3; bim[3][n & 3] = v3;
        } else if (n < 16) {
          const float a = (float)(k1 * (16 * ((n >> 2) & 1) + 4 * q + (n & 3))) * (1.f / 2048.f);
          float x = cosr(a), y = sinr(a); pin(x); pin(y);
          twc[(n >> 2) & 1][n & 3] = x; tws[(n >> 2) & 1][n & 3] = y;
        } else if (n < 20) {
          const float a = (float)(c * (4 * q + (n & 3))) * (1.f / 32.f);
          float x = cosr(a), y = sinr(a); pin(x); pin(y);
          co[n & 3] = x; si[n & 3] = y;
        } else if (n == 20) {
          const unsigned sg = (c & 1) ? 0x80008000u : 0u;
          const unsigned c01 = pk2(co[0], co[1]), c23 = pk2(co[2], co[3]), s01 = pk2(si[0], si[1]), s23 = pk2(si[2], si[3]);
          c32[0] = u32x4{c01, c23, c01 ^ sg, c23 ^ sg}; s32[0] = u32x4{s01, s23, s01 ^ sg, s23 ^ sg};
          pin(c32[0]); pin(s32[0]);
        } else if (n == 21) {
          c32[1] = c32[0] ^ 0x80000000u; s32[1] = s32[0] ^ 0x80000000u; pin(c32[1]); pin(s32[1]);
        } else if (n == 22) {
          n32[0] = s32[0] ^ 0x80008000u; n32[1] = s32[1] ^ 0x80008000u; pin(n32[0]); pin(n32[1]);
        }
      };
      const int hid = 2 * rb + half;
      if (H) {
        const unsigned fo = (16 * (hid >> 3) + 256 * (hid & 7) + 64 * wi) * 514;
#pragma unroll
        for (int a = 0; a < 2; a++) {
          const rsrc_t rF = mk_rsrc(a ? pwi : pwr, WF_BYTES);
#pragma unroll
          for (int n = 0; n < 8; n++) {
            if (n & 3) dma16n(rF, (n & 3) * 1024, lane * 16, fo + (n & 4) * 1024); else dma16(rF, lds + 2 * wi * FRUN, a * FRUN + n * 1024, lane * 16, fo + n * 1024);
          }
          if (lane < 2) dma16(rF, lds + 2 * wi * FRUN, a * FRUN + 8192, lane * 16, fo + 8192);
        }
      }
      stage(0, [&](int i) { if (i > 3) cjob(i - 4); });
      stage(1, [&](int i) { cjob(8 + i); });
      unsigned ev = ald32s(rS, SY_E, 0);
      cjob(20); cjob(21); cjob(22);
      unsigned ca = lds + 16384 + (rb & 2) * 8192 + (((c >> 1) & 3) + 8 * (c >> 3) + 4 * (q & 1) + 16 * (q >> 1)) * 256 +
                    (((4 * (rb & 1) + wi) ^ (c >> 1)) << 5) + (c & 1) * 16;
      unsigned cg = (lane & 24) ? OOB : SY_CG + cb * 8192 + (64 * (rb & 3) + 16 * wi + 8 * (lane >> 5) + c) * 32 + (rb >> 2) * 8;
      u32x4 sel, cr[2];
      f32x4 cs = z4;
      auto col_job = [&](int s, int i) {
        if (H) return;
        if (i == 1) { cr[0] = lds128(ca + s * STG); cr[1] = lds128(ca + s * STG + 8192); }
        if (i == 4) { PRIO(3); cs = mfma(cr[0], sel, cs); cs = mfma(cr[1], sel, cs); pin(cs); SB(); PRIO(0); }
      };
      if (!H) {
#pragma unroll
        for (int e = 0; e < 4; e++) sel[e] = ((c >> 1) & 3) == e ? ((c & 1) ? 0x3f800000u : 0x3f80u) : 0u;
        pin(sel); pin(ca); pin(cg);
      }
      unsigned lu = lds + L_U + lane * 16, luw = lu + wi * 2048, uw = lds + L_U + 4 * q * UROW + k1 * 4;
      unsigned to = OFF_T + pid * 8192 + wi * 2048 + lane * 16, fo = lane == 0 ? SY_FT + wi * 1024 + pid * 4 : OOB;
      unsigned fv = SY_FT + (slot >> 6) * 1024 + (64 * wi + lane) * 4;
      unsigned tg = OFF_T + (64 * wi + 16 * q + (c >> 1)) * 8192 + slot * 32 + (c & 1) * 16, wg = OFF_WT + slot * 8192 + wi * 2048 + q * 256 + cp * 16;
      pin(lu); pin(luw); pin(uw); pin(to); pin(fo); pin(fv); pin(tg); pin(wg);
      wait_v<H ? 0 : 13>(); BAR();
      {
        const u32x4 t0 = lds128(luw), t1 = lds128(luw + 1024);
        SB();
        stage(2, [&](int i) {
          if (i == 0) { st128<SC1>(t0, rS, to); st128<SC1>(t1, rS, to + 1024); }
          col_job(0, i);
        });
      }
      if (H) {
        const unsigned od = c & 1, sal = od ? 0x07060504u : 0x05040302u, slo = od ? 0x01000504u : 0x05040100u, shi = slo + 0x02020202u;
#pragma unroll 1
        for (int it = 0; it < 2; it++) {
          const unsigned g = 4 * q + wi + 16 * it;
          const unsigned a0 = lds + c * 514 - 2 * od + 16 * g, aa = a0 + od * FRUN, ab = a0 + FRUN - od * FRUN;
          u32x4 w[8];
#pragma unroll
          for (int e = 0; e < 4; e++) {
            unsigned x[5], y[5];
#pragma unroll
            for (int j = 0; j < 5; j++) { x[j] = *(LDSP unsigned*)(aa + e * 2 * FRUN + j * 4); y[j] = *(LDSP unsigned*)(ab + e * 2 * FRUN + j * 4); }
#pragma unroll
            for (int j = 0; j < 4; j++) {
              const unsigned u = __builtin_amdgcn_perm(x[j + 1], x[j], sal), v = __builtin_amdgcn_perm(y[j + 1], y[j], sal);
              w[2 * j][e] = __builtin_amdgcn_perm(v, u, slo); w[2 * j + 1][e] = __builtin_amdgcn_perm(v, u, shi);
            }
          }
#pragma unroll
          for (int j = 0; j < 8; j++) st128<SC1>(w[j], rS, OFF_WT + (8 * g + j) * 8192 + hid * 256 + c * 16);
        }
      }
      STAMP2(13); wait_v<H ? 0 : 14>(); if (H) meet(); else BAR(); STAMP2(14);
      pin(ev);
      tag = __builtin_amdgcn_readfirstlane(ev) + 1;
      stage(3, [&](int i) {
        if (i == 3) { wait_v<15>(); st32s(tag, rS, fo); }
        col_job(1, i);
      });
      if (!H) {
        STAMP2(15); wait_v<13>(); BAR(); STAMP2(16);
        stage(4, [&](int i) {
          if (i == 0) {
            float v = swap32_add(cs[0] + cs[2], cs[1] + cs[3]);
            v = swap16_add(v, v);
            st64s(u32x2{asu(v), tag}, rS, cg);
          }
        });
        STAMP2(17); wait_v<13>(); BAR(); STAMP2(18);
      } else __builtin_amdgcn_s_sleep(FDLY);
      u32x4 xa[2][4], wv[2], pr, pi;
      {
        unsigned ft = ald32s(rS, fv, 0);
        stage(5, [&](int i) {
          if (i == 5) {
            wait_v<H ? 0 : 6>(); pin(ft);
            if (!__all(ft == tag)) poll<4>(rS, fv, tag);
            STAMP(1);
#pragma unroll
            for (int ta = 0; ta < 2; ta++) xa[ta][0] = ald128s(rS, tg, ta * 65536);
#pragma unroll
            for (int t = 0; t < 2; t++) wv[t] = ald128s(rS, wg, t * 1024);
          }
        });
      }
      STAMP2(19); if (!H) BAR(); STAMP2(20);
      f32x4 yr[2], yi[2];
      auto turn = [&](int ta, int e0, float sg, float (&a)[2], float (&b)[2]) {
        float x[2], y[2];
#pragma unroll
        for (int e = 0; e < 2; e++) { x[e] = yr[ta][e0 + e] * twc[ta][e0 + e]; y[e] = yi[ta][e0 + e] * twc[ta][e0 + e]; pin(x[e]); pin(y[e]); }
#pragma unroll
        for (int e = 0; e < 2; e++) {
          a[e] = __builtin_fmaf(sg * yi[ta][e0 + e], tws[ta][e0 + e], x[e]); b[e] = __builtin_fmaf(-sg * yr[ta][e0 + e], tws[ta][e0 + e], y[e]);
          pin(a[e]); pin(b[e]);
        }
      };
      stage(6, [&](int i) {
        if (i == 1) {
          wait_v<H ? 0 : 8>(); pin(xa[0][0]); pin(xa[1][0]); pin(wv[0]); pin(wv[1]);
#pragma unroll
          for (int ta = 0; ta < 2; ta++) *(LDSP u32x4*)(luw + ta * 1024) = xa[ta][0];
          nx += 4; lds_arrive(lds + L_CN + CN_X, lane);
        }
        if (i == 2) {
          lds_wait(lds + L_CN + CN_X, nx);
#pragma unroll
          for (int j = 0; j < 8; j++) xa[j >> 2][j & 3] = lds128(lu + (j & 3) * 2048 + (j >> 2) * 1024);
        }
        if (i == 4 || i == 5) {
          const int ta = i - 4;
          PRIO(3);
          yr[ta] = yi[ta] = z4;
#pragma unroll
          for (int j = 0; j < 4; j++) { yr[ta] = mfma(xa[ta][j], bre[j], yr[ta]); yi[ta] = mfma(xa[ta][j], bim[j], yi[ta]); }
          SB(); PRIO(0);
        }
        if (i >= 6 && i < 10) {
          float a[2], b[2];
          turn((i - 6) >> 1, (i & 1) * 2, 1.f, a, b);
          pr[i - 6] = pk2(a[0], a[1]); pi[i - 6] = pk2(b[0], b[1]);
          if (i == 9) { pin(pr); pin(pi); }
        }
        if (i == 10) {
          PRIO(3);
#pragma unroll
          for (int t = 0; t < 2; t++) {
            yr[t] = mfma(c32[t], pr, z4); yi[t] = mfma(c32[t], pi, z4);
            yr[t] = mfma(s32[t], pi, yr[t]); yi[t] = mfma(n32[t], pr, yi[t]);
          }
          SB(); PRIO(0);
        }
      });
      STAMP2(21); if (H) meet(); else { wait_v<12>(); BAR(); } STAMP2(22);
      gr = ald64s(rS, grv, 0); g0 = ald128s(rS, gcv, 0); g1 = ald128s(rS, gcv, 16);
      stage(7, [&](int i) {
        if (i < 4) {
          const int t = i >> 1, e0 = (i & 1) * 2;
          float fr[2], fi[2];
#pragma unroll
          for (int e = 0; e < 2; e++) {
            const float a = bflo(wv[t][e0 + e]), b = bfhi(wv[t][e0 + e]);
            float x = yr[t][e0 + e] * a, y = yi[t][e0 + e] * a;
            pin(x); pin(y);
            fr[e] = __builtin_fmaf(-yi[t][e0 + e], b, x); fi[e] = __builtin_fmaf(yr[t][e0 + e], b, y);
            pin(fr[e]); pin(fi[e]);
          }
          pr[i] = pk2(fr[0], fr[1]); pi[i] = pk2(fi[0], fi[1]);
          if (i == 3) { pin(pr); pin(pi); }
        }
        if (i == 4) {
          PRIO(3);
#pragma unroll
          for (int t = 0; t < 2; t++) {
            yr[t] = mfma(c32[t], pr, z4); yi[t] = mfma(c32[t], pi, z4);
            yr[t] = mfma(n32[t], pi, yr[t]); yi[t] = mfma(s32[t], pr, yi[t]);
          }
          SB(); PRIO(0);
        }
        if (i >= 6 && i < 10) {
          const int ta = (i - 6) >> 1, e0 = (i & 1) * 2;
          float a[2], b[2];
          turn(ta, e0, -1.f, a, b);
#pragma unroll
          for (int e = 0; e < 2; e++) *(LDSP unsigned*)(uw + (16 * ta + e0 + e) * UROW) = pk2(a[e], -b[e]);
        }
      });
      STAMP2(23); wait_v<H ? 0 : 12>(); pin(gr); pin(g0); pin(g1);
      if (H) meet(); else { WAIT_L0(); BAR(); } STAMP2(24);
    };
    if (helper) front(Flag<true>()); else front(Flag<false>());
    const rsrc_t rD = mk_rsrc(pwd, W1_BYTES);
    unsigned dv[4];
#pragma unroll
    for (int p = 0; p < 4; p++) dv[p] = (lane >> 3) * 1024 + (((lane & 7) ^ (q << 1)) << 4) + nb * 128 + p * 7168;
    auto dd = [&](int buf, int n) {
      dma16(rD, lds + wi * 12288, bufaddr(buf), dv[0], step(n) * 32768);
#pragma unroll
      for (int p = 1; p < 4; p++) dma16n(rD, p * 1024, dv[p], step(n) * 32768);
    };
    {
      u32x4 xa[2][4];
      f32x4 yr[2], yi[2];
      unsigned ur = lds + L_U + c * UROW + 16 * q;
      pin(ur);
#pragma unroll
      for (int j = 0; j < 8; j++) xa[j >> 2][j & 3] = lds128(ur + 16 * (j >> 2) * UROW + 64 * (j & 3));
      SB();
      dd(0, 9);
      PRIO(3);
#pragma unroll
      for (int ta = 0; ta < 2; ta++) {
        yr[ta] = yi[ta] = z4;
#pragma unroll
        for (int j = 0; j < 4; j++) { yr[ta] = mfma(bre[j], xa[ta][j], yr[ta]); yi[ta] = mfma(bim[j], xa[ta][j], yi[ta]); }
      }
      SB(); PRIO(0);
      dd(1, 10);
      const float sc = slot == 255 ? 1.f / 1048576.f : 1.f / 524288.f;
#pragma unroll
      for (int ta = 0; ta < 2; ta++) {
        u32x4 o;
#pragma unroll
        for (int e = 0; e < 4; e++) { float a = yr[ta][e] * sc, b = yi[ta][e] * sc; pin(a); pin(b); o[e] = pk2(a, b); }
        st128<SC1>(o, rS, OFF_Z + slot * 8192 + wi * 2048 + q * 512 + c * 16 + ta * 256);
      }
      SB();
      dd(2, 11);
#pragma unroll 1
      for (int it = 0; it < SPIN; it++) {
        if (helper || __all(gr[1] == tag && g0[1] == tag && g0[3] == tag && g1[1] == tag && g1[3] == tag)) break;
        __builtin_amdgcn_s_sleep(2);
        asm volatile("buffer_load_dwordx2 %0, %3, %5, 0 offen sc1\n\tbuffer_load_dwordx4 %1, %4, %5, 0 offen sc1\n\t"
                     "buffer_load_dwordx4 %2, %4, %5, 0 offen offset:16 sc1\n\ts_waitcnt vmcnt(0)"
                     : "=&v"(gr), "=&v"(g0), "=&v"(g1) : "v"(grv), "v"(gcv), "s"(rS) : "memory");
      }
      const float sd = bflo(gr[0]), rr = __builtin_amdgcn_rcpf(sd);
      float s1 = -2.302208198f * rr, s2 = rr * rr, s3 = sd * sd;
      pin(s1); pin(s2); pin(s3);
      s2 *= 0.044715f; pin(s2); s2 *= s1; pin(s2);
      *(LDSP f32x4*)(lds + L_ST + (32 * wi + ((lane & 31) ^ (16 * half))) * 16) = f32x4{bfhi(gr[0]), s1, s2, s3};
      *(LDSP float*)(lds + L_CS + (64 * wi + lane) * 4) = add1(add1(asf(g0[0]), asf(g0[2])), add1(asf(g1[0]), asf(g1[2])));
    }
    STAMP2(25); wait_v<14>(); WAIT_L0(); if (!helper) BAR();
    STAMP(2); CLK(29);
    u32x4 Bq[5][4];
    const unsigned tb = tbase();
    dd(3, 12); dd(4, 13);
    wait_v<12>();
    st32s(tag, rS, lane == 0 ? SY_FZ + wi * 1024 + slot * 4 : OOB);
    STAMP(3);
    dd(5, 0);
    wait_v<13>();
#pragma unroll
    for (int j = 0; j < 3; j++) ldb(Bq[j], tb, j);
    WAIT_L0();
#pragma unroll
    for (int j = 0; j < 12; j++) pin(Bq[j >> 2][j & 3]);
    dd(0, 1); dd(1, 2); dd(2, 3);
    wait_v<16>();
#pragma unroll
    for (int j = 3; j < 5; j++) ldb(Bq[j], tb, j);
    WAIT_L0();
#pragma unroll
    for (int j = 12; j < 20; j++) pin(Bq[j >> 2][j & 3]);
    dd(3, 4); dd(4, 5);
    STAMP(4);
    {
      const unsigned fv = SY_FZ + (rb >> 2) * 1024 + (64 * wi + lane) * 4;
      unsigned zf = ald32s(rS, fv, 0);
      lds_wait(lds + L_CN + CN_S, 4);
      dd(6, 6); dd(7, 7);
      wait_v<8>(); pin(zf);
      if (!__all(zf == tag)) {
        if (helper) __builtin_amdgcn_s_sleep(ZDLY);
        poll<6>(rS, fv, tag);
      }
      lds_set(cLM, 1);
    }
    STAMP(5);
    dd(8, 8);
    res = ald128(rX, obase());
    unsigned hv[4];
#pragma unroll
    for (int p = 0; p < 4; p++) { hv[p] = hbase(p); pin(hv[p]); }
    STAMP(6);
    lds_wait(cML, 1);
    if (helper) __builtin_amdgcn_s_sleep(HDLY);
    __builtin_amdgcn_s_sleep(PDLY);
    STAMP(7);
    poll<0>(rS, lane < 14 ? SY_FH + (rb * 4 + wi) * 2048 + lane * 128 : OOB, tag);
    lds_set(cLM, 2);
    STAMP(8);
    {
      u32x4 A[4][5];
#pragma unroll
      for (int p = 0; p < 4; p++)
#pragma unroll
        for (int j = 0; j < 5; j++) A[p][j] = ald128s(rS, hv[p], step(9 + j) * 4096);
      SB();
      f32x4 a2[4][4];
#pragma unroll
      for (int p = 0; p < 4; p++) {
#pragma unroll
        for (int j = 0; j < 5; j++) {
          const int n = 19 - 5 * p - j;
          asm volatile("s_waitcnt vmcnt(%0)" :: "i"(n) : "memory");
          pin(A[p][j]);
#pragma unroll
          for (int v = 0; v < 4; v++) a2[p][v] = mfma(Bq[j][v], A[p][j], j ? a2[p][v] : z4);
          SB();
          if (p == 1 && j == 0) lds_wait(cML, 2);
          if (p && j < 4) *(LDSP f32x4*)(lds + wi * 12288 + bufaddr(p == 1 ? 5 : p - 2) + j * 1024 + lane * 16) = a2[p - 1][j];
          SB();
        }
      }
#pragma unroll
      for (int v = 0; v < 2; v++) *(LDSP f32x4*)(lds + wi * 12288 + bufaddr(2) + v * 1024 + lane * 16) = a2[3][v];
      o0 = a2[3][2]; o1 = a2[3][3];
    }
    STAMP(9); CLK(30);
  }
  pin(res);
  WAIT_L0();
  STAMP(10);
  BAR();
  STAMP(11);
  if (bid == 0 && wave == 0) st32s(tag, rS, lane == 0 ? SY_E : OOB);
  {
    const unsigned ra = lds + (wave >> 2) * 2048 + lane * 16;
    const unsigned a = ra + wi * 12288 + bufaddr(2);
    o0 += ldsf4(a); o1 += ldsf4(a + 1024);
#pragma unroll
    for (int y = 1; y < 4; y++) {
      const int p = 3 ^ y;
      const unsigned b = ra + (wi ^ y) * 12288, bm = bufaddr(p == 2 ? 6 : 3 + p), bl = bufaddr(p == 0 ? 5 : p - 1);
      o0 += ldsf4(b + bm) + ldsf4(b + bl); o1 += ldsf4(b + bm + 1024) + ldsf4(b + bl + 1024);
    }
    float lo[4], hi[4];
#pragma unroll
    for (int e = 0; e < 4; e++) { const auto r = __builtin_amdgcn_permlane16_swap(asu(o0[e]), asu(o1[e]), false, false); lo[e] = asf(r[0]); hi[e] = asf(r[1]); }
    st128<OAUX>(u32x4{pk2(lo[0] + bflo(res[0]), lo[1] + bfhi(res[0])), pk2(lo[2] + bflo(res[1]), lo[3] + bfhi(res[1])),
                   pk2(hi[0] + bflo(res[2]), hi[1] + bfhi(res[2])), pk2(hi[2] + bflo(res[3]), hi[3] + bfhi(res[3]))}, mk_rsrc(pout, X_BYTES), obase());
  }
  STAMP(12);
  FLUSH();
}

__global__ void ka(char* ws) { unsigned t = (unsigned)__builtin_amdgcn_s_memrealtime(); if (threadIdx.x == 0) *(unsigned*)(ws + (72u << 20)) = t; }
__global__ void kc(char* ws) { unsigned t = __builtin_amdgcn_readfirstlane(*(volatile unsigned*)(ws + (72u << 20))); while ((unsigned)__builtin_amdgcn_s_memrealtime() - t < 4000) __builtin_amdgcn_s_sleep(4); }
extern "C" void kernel_launch(void* const* d_in, const int* in_sizes, int n_in, void* d_out, int out_size, void* d_ws, size_t ws_size,
                              hipStream_t stream) {
  (void)in_sizes; (void)n_in; (void)out_size; (void)ws_size;
  (void)hipFuncSetAttribute((const void*)fused, hipFuncAttributeMaxDynamicSharedMemorySize, LDS_BYTES);
  fused<<<256, 512, LDS_BYTES, stream>>>(d_in[0], d_in[1], d_in[2], d_in[4], d_in[5], d_in[6], (char*)d_ws, d_out);
  kc<<<1, 64, 0, stream>>>((char*)d_ws);
}
```

```cpp
#include <hip/hip_runtime.h>
#include <hip/hip_bf16.h>
#pragma clang diagnostic ignored "-Wint-to-pointer-cast"

typedef __bf16 bf16x8 __attribute__((ext_vector_type(8)));
typedef __bf16 bf16x2 __attribute__((ext_vector_type(2)));
typedef short s16x4 __attribute__((ext_vector_type(4)));
typedef float f32x4 __attribute__((ext_vector_type(4)));
typedef float f32x2 __attribute__((ext_vector_type(2)));
typedef unsigned u32x4 __attribute__((ext_vector_type(4)));
typedef unsigned u32x2 __attribute__((ext_vector_type(2)));
typedef unsigned long long u64x8 __attribute__((ext_vector_type(8)));
#define LDSP __attribute__((address_space(3)))
#define DI __device__ __forceinline__
typedef __amdgpu_buffer_rsrc_t rsrc_t;

constexpr unsigned X_BYTES = 2048u * 512u * 2u;
constexpr unsigned W1_BYTES = 512u * 1792u * 2u;
constexpr unsigned WF_BYTES = 2048u * 257u * 2u;
#define PAD 128
constexpr unsigned TP = 4096u + PAD, CP = 2 * TP;
constexpr unsigned HB = 56u * 4096u, OFF_T = 32u * HB, OFF_Z = OFF_T + 256u * CP, OFF_WT = OFF_Z + 256u * CP, OFF_SY = OFF_WT + 256u * CP;
constexpr unsigned SY_E = OFF_SY, SY_FZ = OFF_SY + 8192, SY_RG = OFF_SY + 16384, SY_CG = OFF_SY + 32768, SY_FH = SY_CG + 14u * 8192u,
                   SY_FT = SY_FH + 64u * 2048u, SY_FW = SY_FT + 65536u, WS_BYTES = SY_FW + 32768u;
constexpr unsigned OOB = 0x40000000u;
constexpr int SC1 = 16;
#define SPINV (1 << 20)
constexpr int SPIN = SPINV;
#define OAUX 16
#define FDLY 6
#define WIN 3
#define PDLY 6
#define HDLY 24
#define EXP 0
#define ZNAP 8
#define THR 4
#define THR2 8
#define ROT 3
#define WRM 1
#define TH4 4
#define PKN 2
#define BJ 0
#define FDL2 0
#define WL0 2
#define WL1 4
#define S1A 12
#define DDP 3
#define FNAP 2
#define HPR 1
#define THA 63
#define THB 63
#define CPS 128
#define CPF 4
#define CB 32256
constexpr unsigned CODE_BYTES = CB;
#define GV 1
#define LW 6
#define ZDLY 8
#define TCA 0
#define TCB 0
#define GATE 2
#define WF 1
#define P1 2
#define LPR 3
#define GA 9
#define GT 7
#define HBP 1
#define LP2 3
#define PKR 0
#define ZS 5
#define ES 1
#define C3 0
#define RM 2
#define SD 0
#define GAL 1
#define TOP 2
#define LPG 3
#define LPH 0
#define FLP 0
#define MP 3
#define OWN 1
#define GP 1
#define LPA 3
#define EM 0
#define EL 0
#define WA 4
#define XL 0
#define CPG 0
#define ZL 1
#define FTI 7
#define HAL 1
#define HT 0
#define HF 0
#define HP 0
#define HG 0
#define EMD 0
#define HQ 1
#define KPAD 512
#define HNAP 2
constexpr unsigned STG = 49152, L_U = 3 * STG, L_ST = L_U + 8192, L_CS = L_ST + 2048, L_CN = L_CS + 1024, L_DG = L_CN + 512, LDS_BYTES = 163840;
constexpr unsigned CN_R = 0, CN_S = 48, CN_LM = 16, CN_ML = 32, CN_T = 64;
constexpr unsigned FRUN = 8224, L_TB = 2 * STG - 24576;

DI rsrc_t mk_rsrc(const void* p, unsigned bytes) { return __builtin_amdgcn_make_buffer_rsrc((void*)p, 0, bytes, 0x00020000); }
DI unsigned pk2(float a, float b) { bf16x2 v = {(__bf16)a, (__bf16)b}; return __builtin_bit_cast(unsigned, v); }
DI unsigned pk2n(float a, float b) { a = -a; asm("" : "+v"(a)); return pk2(a, b); }
DI f32x4 twice(f32x4 a, f32x4 b) { return __builtin_elementwise_fma(a, f32x4{-2.f, -2.f, -2.f, -2.f}, b); }
DI float bflo(unsigned u) { return __builtin_bit_cast(float, u << 16); }
DI float bfhi(unsigned u) { return __builtin_bit_cast(float, u & 0xffff0000u); }
DI float rbf(float a) { return (float)(__bf16)a; }
DI float asf(unsigned u) { return __builtin_bit_cast(float, u); }
DI unsigned asu(float f) { return __builtin_bit_cast(unsigned, f); }
DI f32x4 mfma(u32x4 a, u32x4 b, f32x4 c) {
  return __builtin_amdgcn_mfma_f32_16x16x32_bf16(__builtin_bit_cast(bf16x8, a), __builtin_bit_cast(bf16x8, b), c, 0, 0, 0);
}
DI float cosr(float rev) { return __builtin_amdgcn_cosf(rev); }
DI float sinr(float rev) { return __builtin_amdgcn_sinf(rev); }
template <int N> DI void wait_v() { asm volatile("s_waitcnt vmcnt(%0)" :: "i"(N) : "memory"); }
#define WAIT_L0() asm volatile("s_waitcnt lgkmcnt(0)" ::: "memory")
#define BAR() do { asm volatile("" ::: "memory"); __builtin_amdgcn_s_barrier(); asm volatile("" ::: "memory"); } while (0)
#define SB() __builtin_amdgcn_sched_barrier(0)
#define AL() do { SB(); asm volatile(".p2align 3"); SB(); } while (0)
template <int N> struct Cn { static constexpr int v = N; };
template <int A, int B, class F> DI void sfor(F&& f) { if constexpr (A < B) { f(Cn<A>{}); sfor<A + 1, B>(f); } }
#define EACH(n) [&](auto n##_) __attribute__((always_inline))
template <class T> DI void pin(T& v) { asm volatile("" : "+v"(v)); }
DI void dma16(rsrc_t r, unsigned la, unsigned li, unsigned voff, unsigned soff) {
  asm volatile("s_add_i32 m0, %0, %4\n\ts_nop 0\n\tbuffer_load_dwordx4 %1, %2, %3 offen lds" :: "s"(la), "v"(voff), "s"(r), "s"(soff), "i"(li) : "memory", "scc");
}
DI u32x4 ald128s(rsrc_t r, unsigned v, unsigned s) { u32x4 o; asm volatile("buffer_load_dwordx4 %0, %1, %2, %3 offen sc1" : "=v"(o) : "v"(v), "s"(r), "s"(s) : "memory"); return o; }
DI u32x2 ald64s(rsrc_t r, unsigned v, unsigned s) { u32x2 o; asm volatile("buffer_load_dwordx2 %0, %1, %2, %3 offen sc1" : "=v"(o) : "v"(v), "s"(r), "s"(s) : "memory"); return o; }
DI unsigned ald32s(rsrc_t r, unsigned v, unsigned s) { unsigned o; asm volatile("buffer_load_dword %0, %1, %2, %3 offen sc1" : "=v"(o) : "v"(v), "s"(r), "s"(s) : "memory"); return o; }
DI u32x4 ld128s(rsrc_t r, unsigned v, unsigned s) { return __builtin_amdgcn_raw_buffer_load_b128(r, v, s, SC1); }
template <int AUX> DI void st128(u32x4 v, rsrc_t r, unsigned voff) { __builtin_amdgcn_raw_buffer_store_b128(v, r, voff, 0, AUX); }
DI void st64s(u32x2 v, rsrc_t r, unsigned voff) { __builtin_amdgcn_raw_buffer_store_b64(v, r, voff, 0, SC1); }
DI void st32s(unsigned v, rsrc_t r, unsigned voff) { __builtin_amdgcn_raw_buffer_store_b32(v, r, voff, 0, SC1); }
DI u32x4 lds128(unsigned a) { return *(LDSP u32x4*)a; }
DI f32x4 ldsf4(unsigned a) { return *(LDSP f32x4*)a; }
DI u32x2 ldstr(unsigned a) { return __builtin_bit_cast(u32x2, __builtin_amdgcn_ds_read_tr16_b64_v4i16((LDSP s16x4*)a)); }
template <int D> DI u32x4 ldstr2(unsigned a) { const u32x2 lo = ldstr(a), hi = ldstr(a + D); return u32x4{lo[0], lo[1], hi[0], hi[1]}; }
template <int C> DI float dpp_add(float v) { return v + asf(__builtin_amdgcn_update_dpp(0, asu(v), C, 0xf, 0xf, true)); }
DI float swap16_add(float a, float b) { const auto r = __builtin_amdgcn_permlane16_swap(asu(a), asu(b), false, false); return asf(r[0]) + asf(r[1]); }
DI float swap32_add(float a, float b) { const auto r = __builtin_amdgcn_permlane32_swap(asu(a), asu(b), false, false); return asf(r[0]) + asf(r[1]); }
DI float sum16(float v) { v = dpp_add<0xB1>(v); v = dpp_add<0x4E>(v); v = dpp_add<0x141>(v); return dpp_add<0x128>(v); }
DI float dot2(unsigned a, unsigned b, float s) { return __builtin_amdgcn_fdot2_f32_bf16(__builtin_bit_cast(bf16x2, a), __builtin_bit_cast(bf16x2, b), s, false); }
template <int B> DI void swp(int w, u32x4& a, u32x4& b) {
  unsigned x0 = a[0], x1 = a[1], x2 = a[2], x3 = a[3], y0 = b[0], y1 = b[1], y2 = b[2], y3 = b[3];
  asm volatile("s_bitcmp0_b32 %8, %9\n\ts_cbranch_scc1 1f\n\tv_swap_b32 %0, %4\n\tv_swap_b32 %1, %5\n\tv_swap_b32 %2, %6\n\tv_swap_b32 %3, %7\n1:"
               : "+v"(x0), "+v"(x1), "+v"(x2), "+v"(x3), "+v"(y0), "+v"(y1), "+v"(y2), "+v"(y3) : "s"(w), "i"(B) : "scc");
  a = u32x4{x0, x1, x2, x3}; b = u32x4{y0, y1, y2, y3};
}
DI float add1(float a, float b) { float o; asm("v_add_f32 %0, %1, %2" : "=v"(o) : "v"(a), "v"(b)); return o; }
template <int NAP> DI void poll(rsrc_t r, unsigned voff, unsigned tag) {
#pragma unroll 1
  for (int it = 0; it < SPIN; it++) {
    unsigned v = ald32s(r, voff, 0);
    wait_v<0>(); pin(v);
    if (__all(voff == OOB || v == tag)) break;
    __builtin_amdgcn_s_sleep(NAP);
  }
}
DI void lds_wait(unsigned a, unsigned n) {
#pragma unroll 1
  for (int it = 0; it < SPIN * 8; it++) { if (*(volatile LDSP unsigned*)a >= n) break; __builtin_amdgcn_s_sleep(1); }
  asm volatile("" ::: "memory");
}
DI void lds_bits(unsigned a, unsigned m) {
#pragma unroll 1
  for (int it = 0; it < SPIN * 8; it++) { if ((*(volatile LDSP unsigned*)a & m) == m) break; __builtin_amdgcn_s_sleep(1); }
  asm volatile("" ::: "memory");
}
DI void lds_wait4(unsigned a) {
#pragma unroll 1
  for (int it = 0; it < SPIN * 8; it++) { const u32x4 v = *(volatile LDSP u32x4*)a; if (v[0] & v[1] & v[2] & v[3]) break; __builtin_amdgcn_s_sleep(1); }
  asm volatile("" ::: "memory");
}
DI void lds_set(unsigned a, unsigned v) { asm volatile("" ::: "memory"); *(volatile LDSP unsigned*)a = v; asm volatile("" ::: "memory"); }
DI void dma16n(rsrc_t r, unsigned off, unsigned voff, unsigned soff) {
  asm volatile("buffer_load_dwordx4 %0, %1, %2 offen offset:%3 lds" :: "v"(voff), "s"(r), "s"(soff), "i"(off) : "memory");
}
DI u32x4 ald128(rsrc_t r, unsigned v) { u32x4 o; asm volatile("buffer_load_dwordx4 %0, %1, %2, 0 offen" : "=v"(o) : "v"(v), "s"(r) : "memory"); return o; }
DI unsigned ald32(rsrc_t r, unsigned v) { unsigned o; asm volatile("buffer_load_dword %0, %1, %2, 0 offen" : "=v"(o) : "v"(v), "s"(r) : "memory"); return o; }
constexpr int younger(int n) {
  int k = (n + LW < 28 ? n + LW : 28) - n - 1;
  for (int t = 0; t < n; t += 3) k += t + LW >= n && t + LW < 27;
  return k;
}
constexpr unsigned bufaddr(int b) { return (b < 3 ? 2 * STG : b < 6 ? 0 : STG) + (b % 3) * 4096; }
#define PINC() asm volatile("" :: "v"(cpf))
#define PRIO(n) __builtin_amdgcn_s_setprio(n)

#define STAMP(i)
#define CLK(i)
#define FLUSH()
#define CLK3(i)
#define STAMP4(i)
#define STAMP5(i)
#define STAMP2(i)

__device__ __attribute__((used, noinline, aligned(4096))) void lead() { asm volatile(".fill %0, 4, 0xBF800000" :: "i"(KPAD / 4 - 8)); }
__global__ __launch_bounds__(512, 2) void fused(const void*, const void*, const void*, const void*, const void*, const void*, char*, void*) {
  extern __shared__ __attribute__((aligned(16))) char lds_[];
  const unsigned lds = (unsigned)(size_t)(LDSP char*)lds_;
  u64x8 ka;
  asm volatile("s_load_dwordx16 %0, %1, 0" : "=s"(ka) : "s"(__builtin_amdgcn_kernarg_segment_ptr()));
  unsigned cpf = ald32(mk_rsrc((const char*)__builtin_amdgcn_s_getpc() + (blockIdx.x >> 3) % CPF * (CODE_BYTES / CPF), CODE_BYTES / CPF), threadIdx.x * CPS);
  SB();
  const int bid = blockIdx.x, xcd = bid & 7, idx = bid >> 3;
  const int rb = (xcd & 3) * 4 + (idx & 3), half = xcd >> 2, nb = idx >> 2, cb = nb < 7 ? 7 * half + nb : 0;
  const int ai = 8 * half + nb, hid = 2 * rb + half, rbase = 16 * (rb & 1) + 64 * (rb >> 1);
  const bool helper = nb == 7;
  const int wave = __builtin_amdgcn_readfirstlane(threadIdx.x >> 6), wi = wave & 3;
  const unsigned lane = threadIdx.x & 63, c = lane & 15, q = lane >> 4;
  unsigned xv[4], wv4[4];
  auto xset = [&]() {
#pragma unroll
    for (int i = 0; i < 4; i++) {
      xv[i] = 4096 - 1024 * i + (rbase + 512 * wi + 32 * ((i >> 1) ^ half) + (i & 1) * 8 + (lane >> 3)) * 1024 + ((((lane & 7) ^ q) << 4) ^ ((i & 1) * 64));
      wv4[i] = (16 * wi + 4 * i + q) * 3584 - 1024 * i + cb * 256 + (((c ^ (q << 1)) << 4) ^ ((i & 2) * 64));
      pin(xv[i]); pin(wv4[i]);
    }
  };
  unsigned np = rbase + 32 * (wi & 1) + ai, l16 = lane * 16, ev;
  const unsigned ftw = SY_FT + (wi >> 1) * 32768 + (ai >> 1) * 4096 + (2 * rb + (ai & 1)) * 128;
  if (TOP) { if (!XL) xset(); pin(l16); asm volatile("" : "+s"(np)); SB(); }
  asm volatile("s_waitcnt lgkmcnt(0)" : "+s"(ka) : "s"(wave));
  const char *px = (const char*)ka[0], *pwr = (const char*)ka[1], *pwi = (const char*)ka[2], *pwg = (const char*)ka[3], *pwu = (const char*)ka[4],
             *pwd = (const char*)ka[5], *ws = (const char*)ka[6], *pout = (const char*)ka[7];
  const rsrc_t rS = mk_rsrc(ws, WS_BYTES), rX = mk_rsrc(px, X_BYTES);
  const f32x4 z4 = {0.f, 0.f, 0.f, 0.f};
  unsigned tag;
  STAMP(0);

  const bool slow = ROT == 2 || (ROT == 1 && ((((unsigned)(size_t)pwd >> 7) + nb) & 7) == 3) || (ROT == 3 && ((((unsigned)(size_t)pwd >> 7) + nb) & 3) == 3);
  const int rot = (nb + (slow ? RM * (idx & 3) : 0)) % 7, ro1 = helper ? rot : nb, ro2 = (slow && !helper) ? (idx & 3) * 3 >> 1 : 0;
  auto step = [&](int n) {
    const int u = n - 1 + ro2, t = (OWN && n > 0 && n < 8) ? ro1 + (n == 1 ? 0 : u > 6 ? u - 6 : u) : rot + (n + 6) % 7;
    return 4 * ((t >= 7 ? t - 7 : t) + 7 * (n > 0 && n < 8 ? half : 1 - half)) + wi;
  };
  auto tbase = [&]() { return lds + wi * 12288 + (4 * q + (c >> 2)) * 128 + (((2 * q + (c >> 3)) & 3) << 5) + (c & 3) * 8; };
  auto hbase = [&](int p) { return (2 * rb + half) * HB + ((p ^ 3 ^ wi) << 10) + lane * 16; };
  auto obase = [&]() { return (rbase + 512 * wi + 32 * half + c) * 1024 + nb * 128 + (wave >> 2) * 64 + (q & 1) * 32 + (q >> 1) * 16; };
  auto ftpos = [&](unsigned n) { return n / PKN * 128 + n % PKN * 4; };
  auto ftflag = [&]() { st32s(tag, rS, lane == 0 ? ftw + (wi & 1) * 4 : OOB); };
  const unsigned cLM = lds + L_CN + CN_LM + wi * 4, cML = lds + L_CN + CN_ML + wi * 4;
  const rsrc_t rD = mk_rsrc(pwd, W1_BYTES);
  unsigned dv[4];
  auto dset = [&]() {
#pragma unroll
    for (int p = 0; p < 4; p++) { dv[p] = (lane >> 3) * 1024 + (((lane & 7) ^ (q << 1)) << 4) + nb * 128 + p * 7168; pin(dv[p]); }
  };
  auto dd = [&](int buf, int n) {
    dma16(rD, lds + wi * 12288, bufaddr(buf), dv[0], step(n) * 32768);
#pragma unroll
    for (int p = 1; p < 4; p++) dma16n(rD, p * 1024, dv[p], step(n) * 32768);
  };
  f32x4 o0, o1;

  const rsrc_t rG = mk_rsrc(pwg, W1_BYTES), rU = mk_rsrc(pwu, W1_BYTES), rB = mk_rsrc((const char*)px - 4096, X_BYTES + 4096);
  auto stage = [&](int s, auto&& job) {
    const int ks = (s + 2 * (rb >> 2)) & 7;
#pragma unroll
    for (int i = 0; i < 12; i++) {
      const rsrc_t r = i < 4 ? rB : i < 8 ? rG : rU;
      const unsigned vo = i < 4 ? xv[i] : wv4[i & 3], so = i < 4 ? ks * 128 : ks * (64 * 3584);
      if (i & 3) dma16n(r, (i & 3) * 1024, vo, so); else dma16(r, lds + wi * 4096, (s % 3) * STG + (i >> 2) * 16384, vo, so);
      job(i); SB();
    }
  };
  auto none = [&](int) {};
  u32x4 mr[4], mi[4];
  auto first = [&]() {
    const float m = (float)c * (1.f / 512.f), n = (float)(2 * wave + 128 * q);
#pragma unroll
    for (int i = 0; i < 4; i++) {
#pragma unroll
      for (int e = 0; e < 4; e++) { const float a = (n + (float)((i >> 1) + 16 * (i & 1) + 32 * e)) * m, u = cosr(a), v = sinr(a); mr[i][e] = pk2(u, v); mi[i][e] = pk2n(v, u); }
      pin(mr[i]); pin(mi[i]);
    }
  };

  u32x4 G[4][4], own[4];
  unsigned fh, fl;
  auto gen = [&](int v) {
    const float last = (wi == 3 && q == 3) ? 0.5f : 1.f;
    const float d = (float)(64 * nb + 16 * v + c) * (1.f / 512.f), a = d * (float)(64 * wi + 16 * q);
    f32x2 u0 = {cosr(a), sinr(a)}, u1 = {cosr(a + d), sinr(a + d)};
    const float h = cosr(d);
    pin(u0); pin(u1);
    SB();
    const float k = h + h;
    asm volatile(".p2align 3" :: "v"(k));
    SB();
#pragma unroll
    for (int j = 0; j < 16; j++) {
      if (j == 15) u1 *= last;
      G[j >> 2][v][j & 3] = pk2(u1[0], u1[1]);
      const f32x2 u2 = k * u1 - u0; u0 = u1; u1 = u2;
      if ((j & 3) == 3) pin(G[j >> 2][v]);
    }
    SB();
  };
  auto done = [&](unsigned ff, bool bar) {
    CLK(28); STAMP(3);
    if (HQ && bar) PRIO(0);
#pragma unroll
    for (int v = 0; v <= (bar && HG ? 3 : GV); v++) gen(v);
    fh = (lane < 7 && lane != nb) ? SY_FH + (rb * 4 + wi) * 2048 + (7 * half + lane) * 128 : OOB;
    pin(ff); pin(fh);
    if (EM == 3 && bar) { __builtin_amdgcn_s_sleep(EMD); fl = ald32s(rS, fh, 0); }
    wait_v<0>();
    if (bar) BAR();
    st32s(tag, rS, ff);
    if (EM == 2 && bar) fl = ald32s(rS, fh, 0);
    if (EM && !bar) asm volatile("" : "=v"(fl));
    if (HG && !bar) {
#pragma unroll
      for (int i = 4 * GV + 4; i < 16; i++) asm volatile("" : "=v"(G[i & 3][i >> 2]));
    }
    lds_set(cML, 6);
    if (GP && !bar) PRIO(0);
    STAMP(4);
    SB();
  };
  auto rows = [&]() {
    PRIO(3);
    dma16(rX, lds + L_U + (wi & 1) * 4096, 0, l16, np * 1024);
#pragma unroll
    for (int j = 1; j < 4; j++) dma16n(rX, j * 1024, l16, (np + 511 * j) * 1024);
    asm volatile("buffer_load_dword %0, off, %1, %2 sc1" : "=v"(ev) : "s"(rS), "s"(SY_E) : "memory");
    PRIO(HPR);
  };
  auto filt = [&]() {
      const unsigned fo = (64 * hid + 16 * wi) * 514;
#pragma unroll
      for (int a = 0; a < 2; a++) {
        const rsrc_t rF = mk_rsrc(a ? pwi : pwr, WF_BYTES);
#pragma unroll
        for (int n = 0; n < 8; n++) {
          if (n & 3) dma16n(rF, (n & 3) * 1024, l16, fo + (n & 4) * 1024); else dma16(rF, lds + wi * FRUN, a * 4 * FRUN + n * 1024, l16, fo + n * 1024);
        }
        if (lane < 2) dma16(rF, lds + wi * FRUN, a * 4 * FRUN + 8192, l16, fo + 8192);
      }
  };
  if (TOP) {
    int w2 = wave;
    asm volatile("" : "+s"(w2));
    if (w2 & 4) rows();
    SB();
    if (XL) xset();
    w2 += 8 * helper;
    asm volatile("" : "+s"(w2));
    if (HT == 2) { if ((w2 >> 2) == 2) filt(); asm volatile("" : "+s"(w2)); }
    if (w2 < 4) { stage(0, none); if (TOP > 1) stage(1, none); }
    if (HT == 1) { asm volatile("" : "+s"(w2)); if ((w2 >> 2) == 2) filt(); }
  }

  if (wave < 4) {
    if (!helper) {
      if (!TOP) { xset(); stage(0, none); }
      unsigned xa0 = lds + c * 128 + ((q ^ (c >> 1)) << 4), xa1 = xa0 ^ 64;
      unsigned wa0 = lds + 16384 + (8 * q + (c >> 2)) * 256 + (((2 * wi) ^ ((c >> 2) | ((q & 1) << 2))) << 5) + (c & 3) * 8, wa1 = wa0 ^ 32;
      unsigned ac = lds + L_CS + 128 * wi + 16 * q, as = lds + L_ST + c * 16;
      pin(xa0); pin(xa1); pin(wa0); pin(wa1); pin(ac); pin(as);
      if (S1A == 0) BAR();
      if (TOP < 2) stage(1, [&](int i) { if (i == S1A - 1 && S1A < 12) BAR(); });
      const unsigned wt = lane < 16 ? ((rb * 14 + cb) * 4 + wi) * 2048 + lane * 128 : (C3 && lane < 16 + 16 * C3) ? ((idx * 4 + wi) * 16 + c) * 1024 + (((q == 1 ? 384 : 896) - (unsigned)(size_t)pwd) & 896) : OOB;
      if (WRM) asm volatile("s_mov_b32 m0, %0\n\ts_nop 0\n\tbuffer_load_dword %1, %2, 0 offen lds" :: "s"(lds + L_CN + 256), "v"(wt), "s"(rD) : "memory");
      if (WF) asm volatile("buffer_load_dword %0, %1, 0 offen sc1 lds" :: "v"(lane == 0 ? SY_FH + (rb * 4 + wi) * 2048 + cb * 128 : (WF > 1 && lane < 3) ? (cb * 4 + wi) * 4096 + (2 * rb - 1 + lane) * HB : OOB), "s"(rS) : "memory");
      PRIO(2);
      f32x4 acc[8][4];
      u32x4 W[2][4], X[2][4];
      auto lw = [&](int b, int m0, int ks) {
#pragma unroll
        for (int m = m0; m < m0 + 2; m++) W[b][m] = ldstr2<1024>(((m & 1) ? wa1 : wa0) + ks * 8192 + (m >> 1) * 16384);
      };
      auto lx = [&](int h, int ks) {
#pragma unroll
        for (int t = 0; t < 4; t++) X[h][t] = lds128((ks ? xa1 : xa0) + (h * 4 + t) * 2048);
      };
      auto mm = [&](int b, int h, int t0, int t1, bool first = false) {
#pragma unroll
        for (int t = t0; t < t1; t++)
#pragma unroll
          for (int m = 0; m < 4; m++) acc[h * 4 + t][m] = mfma(W[b][m], X[h][t], first ? z4 : acc[h * 4 + t][m]);
#pragma unroll
        for (int i = 0; i < (t1 - t0) * 4; i++) { __builtin_amdgcn_sched_group_barrier(0x008, 1, 0); __builtin_amdgcn_sched_group_barrier(0x102, 1, 0); }
        SB();
      };
      auto front = [&](unsigned dl, bool first) {
        lx(1, 0); lw(1, 0, 1); mm(0, 0, 0, 4, first);
        lw(1, 2, 1); lx(0, 1); mm(0, 1, 0, 4, first);
        lx(1, 1); xa0 += dl; xa1 += dl; wa0 += dl; wa1 += dl; mm(1, 0, 0, 3);
        pin(xa0); pin(xa1); pin(wa0); pin(wa1);
      };
      wait_v<12 + WRM + !!WF>(); BAR();
      PINC();
      STAMP(1); CLK(29);
      lw(0, 0, 0); lx(0, 0); lw(0, 2, 0);
      SB();
      front(STG, true);
      wait_v<WRM + !!WF>();
      int sl = 1;
#pragma unroll 1
      for (int s = 1; s < 8; s++) {
        const unsigned dl = (sl == 2) ? -2 * STG : STG;
        sl = (sl == 2) ? 0 : sl + 1;
        STAMP2(14 + s);
        WAIT_L0(); BAR();
        lw(0, 0, 0); mm(1, 0, 3, 4);
        lw(0, 2, 0); lx(0, 0); mm(1, 1, 0, 4);
        front(dl, false);
      }
      f32x4 cs[4], st[8];
      WAIT_L0();
      lds_set(lds + L_CN + CN_S + wi * 4, 1);
      const unsigned tv = *(LDSP unsigned*)(lds + L_CN + CN_T);
      st[0] = ldsf4(as);
#pragma unroll
      for (int m = 0; m < 4; m++) cs[m] = ldsf4(ac + (m >> 1) * 512 + 64 * (m & 1));
      mm(1, 0, 3, 4);
#pragma unroll
      for (int t = 1; t < 8; t++) st[t] = ldsf4(as + 256 * t);
      mm(1, 1, 0, 4);
      tag = __builtin_amdgcn_readfirstlane(tv);
      STAMP(2); CLK(30);
      const unsigned ho = (cb * 4 + wi) * 4096 + lane * 16;
      unsigned ho0 = ho + (2 * rb + half) * HB, ho1 = ho + (2 * rb + 1 - half) * HB;
      f32x2 sh[8];
#pragma unroll
      for (int t = 7; t >= 0; t--) { sh[t] = st[t].hi; if (GAL) pin(sh[t]); }
      if (GAL) {
        pin(ho0); pin(ho1);
        AL();
      }
      u32x4 ho_[8];
#pragma unroll
      for (int t = 0; t < 8; t++) {
        f32x2 g[4], u[4], y[4];
#pragma unroll
        for (int p = 0; p < 4; p++) { const int v = p >> 1, e = (p & 1) * 2; g[p] = f32x2{acc[t][v][e], acc[t][v][e + 1]} - st[t][0] * f32x2{cs[v][e], cs[v][e + 1]}; }
#pragma unroll
        for (int p = 0; p < 4; p++) y[p] = g[p] * g[p];
        if (GAL) AL(); else SB();
#pragma unroll
        for (int p = 0; p < 4; p++) y[p] = st[t][1] + sh[t][1] * y[p];
#pragma unroll
        for (int p = 0; p < 4; p++) y[p] *= g[p];
        SB();
#pragma unroll
        for (int p = 0; p < 4; p++) {
          const int v = p >> 1, e = (p & 1) * 2;
          y[p] = f32x2{__builtin_amdgcn_exp2f(y[p][0]), __builtin_amdgcn_exp2f(y[p][1])};
          u[p] = f32x2{acc[t][2 + v][e], acc[t][2 + v][e + 1]} - st[t][0] * f32x2{cs[2 + v][e], cs[2 + v][e + 1]};
        }
        SB();
#pragma unroll
        for (int p = 0; p < 4; p++) y[p] = sh[t][0] + sh[t][0] * y[p];
        SB();
#pragma unroll
        for (int p = 0; p < 4; p++) { y[p] = f32x2{__builtin_amdgcn_rcpf(y[p][0]), __builtin_amdgcn_rcpf(y[p][1])}; u[p] *= g[p]; }
        SB();
#pragma unroll
        for (int p = 0; p < 4; p++) u[p] *= y[p];
        SB();
        ho_[t] = u32x4{pk2(u[0][0], u[0][1]), pk2(u[1][0], u[1][1]), pk2(u[2][0], u[2][1]), pk2(u[3][0], u[3][1])};
        if (SD) { pin(ho_[t]); SB(); }
        if (t == 7 && SD == 7) CLK(27);
#pragma unroll
        for (int i = 0; i < 8; i++) if (t == (SD == 4 ? 4 + i / 2 : SD == 2 ? (i < 4 ? 2 + i / 2 : i) : SD == 7 ? 7 : SD == 3 ? (i < 6 ? 3 + i / 2 : i) : i)) st128<SC1>(ho_[i], rS, ((i & 1) ? ho1 : ho0) + (i >> 1) * 1024);
        if (GA < 9 && t == GT) lds_set(cML, 4);
      }
      SB();
#pragma unroll
      for (int p = 0; p < 4; p++) own[p] = ho_[2 * (p ^ 3)];
      done(lane ? OOB : SY_FH + (rb * 4 + wi) * 2048 + cb * 128, false);
    } else {
      if (!HT) filt();
      unsigned wa = ald32s(rS, ((WA & 2) && lane < 2) ? SY_FW + (2 * wi + lane) * 4096 + hid * 128 : ((WA & 4) && threadIdx.x == 2) ? SY_FZ + hid * 128 : OOB, 0);
      const float x = (float)((3 ^ (hid >> 3)) * wi + 1) * 0.25f;
      f32x4 tc[2], ts[2];
      u32x4 br, bi, fr, fi;
#pragma unroll
      for (int e = 0; e < 4; e++) {
        const float k = (float)(16 * q + 4 * e + wi), a = k * (float)c, u = cosr(a * (1.f / 64.f)), v = sinr(a * (1.f / 64.f));
        br[e] = pk2(u, v); bi[e] = pk2n(v, u);
#pragma unroll
        for (int ta = 0; ta < 2; ta++) { const float w = a * (1.f / 2048.f) + k * (ta / 128.f) + (x + ta * 0.5f); tc[ta][e] = cosr(w) * (1.f / 524288.f); ts[ta][e] = sinr(w) * (1.f / 524288.f); }
        const float o = (float)((4 * q + e) * (2 * c + 1) + 16) * (1.f / 32.f), f = cosr(o), g = sinr(o);
        fr[e] = pk2(f, g); fi[e] = pk2n(g, f);
      }
      const unsigned a = lds + L_TB + lane * 16, b = a + wi * 6144;
      *(LDSP u32x4*)(a - 2048) = fr; *(LDSP u32x4*)(a - 1024) = fi;
      *(LDSP u32x4*)b = br; *(LDSP u32x4*)(b + 1024) = bi;
#pragma unroll
      for (int ta = 0; ta < 2; ta++) { *(LDSP f32x4*)(b + 2048 + ta * 2048) = tc[ta]; *(LDSP f32x4*)(b + 3072 + ta * 2048) = ts[ta]; }
      if (HF) first();
      wait_v<0>();
      PINC(); pin(wa);
    }
  } else {
    {
      const int hf = wi & 1, h = wi >> 1;
      const unsigned ra = lds + L_U + hf * 4096;
      if (!TOP) rows();
      if (wave == 4 && lane < 16) *(LDSP unsigned*)(lds + L_CN + lane * 4) = 0;
      u32x4 C, N, A2;
      f32x4 tc, ts;
      float cw[3], sw[3];
      {
        float co[4], si[4], ca[4], sa[4];
#pragma unroll
        for (int e = 0; e < 4; e++) {
          const float n1 = (float)(4 * q + e), a = n1 * (float)(c + 1 + 16 * h);
          co[e] = cosr(a * (1.f / 32.f)); si[e] = -sinr(a * (1.f / 32.f));
          tc[e] = cosr(a * (1.f / 512.f)); ts[e] = sinr(a * (1.f / 512.f));
          const float b = n1 * (float)(2 * (c >> 2) + (c & 1)) * (1.f / 16.f) + (float)(c & 2) * 0.125f;
          ca[e] = cosr(b); sa[e] = sinr(b);
        }
#pragma unroll
        for (int r = 0; r < 3; r++) { const float a = (float)np * ((r + 1) / 2048.f); cw[r] = cosr(a); sw[r] = sinr(a); pin(cw[r]); pin(sw[r]); }
        const unsigned sg = (c & 1) ? 0u : 0x80008000u;
        const unsigned c01 = pk2(co[0], co[1]), c23 = pk2(co[2], co[3]), s01 = pk2(si[0], si[1]), s23 = pk2(si[2], si[3]);
        C = u32x4{c01, c23, c01 ^ sg, c23 ^ sg}; N = u32x4{s01, s23, s01 ^ sg, s23 ^ sg};
        A2 = u32x4{pk2(ca[0], ca[1]), pk2(ca[2], ca[3]), pk2(sa[0], sa[1]), pk2(sa[2], sa[3])};
        pin(C); pin(N); pin(A2); pin(tc); pin(ts);
      }
      if (!helper && !TOP) xset();
      f32x2 mk = (h && c == 15) ? f32x2{-1.f, -1.f} : f32x2{0.f, 0.f};
      unsigned go = (c == 0 && (q >> 1) == h) ? SY_RG + (128 * rb + 32 * q + 16 * hf + ai) * 8 : OOB;
      unsigned to = OFF_T + (16 * rb + ai) * CP + hf * TP + (64 * q + 16 * h + c) * 16, xr = ra + lane * 8;
      u32x4 X[4], sel[4];
      float p[4];
#pragma unroll
      for (int n = 0; n < 4; n++) { const unsigned v = (c >> 2) == n ? 0x3f803f80u : 0u; sel[n] = u32x4{v, v, v, v}; pin(sel[n]); p[n] = 0.f; pin(p[n]); }
      pin(mk); pin(go); pin(to); pin(xr);
      wait_v<0>();
      pin(ev); PINC();
      tag = __builtin_amdgcn_readfirstlane(ev) + 1;
      *(LDSP unsigned*)(lds + L_CN + CN_T) = tag;
      STAMP(13);
      f32x4 yr[4], yi[4], t[4], sm = z4;
#pragma unroll
      for (int n = 0; n < 4; n++) X[n] = ldstr2<512>(xr + n * 1024);
#pragma unroll
      for (int n = 0; n < 4; n++) {
        yr[n] = mfma(X[n], C, z4); yi[n] = mfma(X[n], N, z4); sm = mfma(sel[n], X[n], sm);
#pragma unroll
        for (int e = 0; e < 4; e++) p[n] = dot2(X[n][e], X[n][e], p[n]);
      }
      float sq = swap16_add(swap32_add(p[0], p[2]), swap32_add(p[1], p[3])), m = sm[0];
      auto turn = [&](int n) {
        const float k = asf(__builtin_amdgcn_readlane(asu(yr[n][0]), 15));
        const f32x2 lo = mk * k + f32x2{yr[n][0], yr[n][1]}, hi = mk * k + f32x2{yr[n][2], yr[n][3]};
        const f32x4 y = {lo[0], lo[1], hi[0], hi[1]}, a = y * tc + yi[n] * ts, b = yi[n] * tc - y * ts;
        t[n] = mfma(A2, u32x4{pk2(a[0], a[1]), pk2(a[2], a[3]), pk2(b[0], b[1]), pk2(b[2], b[3])}, z4);
      };
      sq = dpp_add<0xB1>(sq); pin(sq); m = dpp_add<0xB1>(m); pin(m); turn(0);
      sq = dpp_add<0x4E>(sq); pin(sq); m = dpp_add<0x4E>(m); pin(m); SB();
      sq = dpp_add<0x141>(sq); pin(sq); m = dpp_add<0x141>(m); pin(m); turn(1);
      sq = dpp_add<0x128>(sq); pin(sq); m = dpp_add<0x128>(m); pin(m); SB();
      m *= 1.f / 512.f;
      const float var = fmaxf(sq * (1.f / 512.f) - m * m, 0.f);
      const unsigned st = pk2(__builtin_amdgcn_sqrtf(rbf(rbf(var) + 1e-5f)), m);
      turn(2);
      const unsigned rr = asu(__builtin_amdgcn_rcpf(bflo(st)));
      turn(3);
      const float r2 = asf(__builtin_amdgcn_readlane(rr, 32)), r3 = asf(__builtin_amdgcn_readlane(rr, 48));
      t[0] *= asf(__builtin_amdgcn_readlane(rr, 0)); t[1] *= asf(__builtin_amdgcn_readlane(rr, 16));
      const f32x4 s0 = t[0] + r2 * t[2], s1 = t[0] - r2 * t[2], s2 = t[1] + r3 * t[3], s3 = t[1] - r3 * t[3];
      const f32x4 d0 = s0 + s2, d2 = s0 - s2;
      const f32x2 re[3] = {s1.lo + s3.hi, d2.lo, s1.lo - s3.hi}, im[3] = {s1.hi - s3.lo, d2.hi, s1.hi + s3.lo};
      u32x4 o[2];
#pragma unroll
      for (int r = 0; r < 4; r++) {
        const f32x2 x = r ? re[r - 1] * cw[r - 1] + im[r - 1] * sw[r - 1] : d0.lo, y = r ? im[r - 1] * cw[r - 1] - re[r - 1] * sw[r - 1] : d0.hi;
        o[0][r] = pk2(x[0], y[0]); o[1][r] = pk2(x[1], y[1]);
      }
      st128<SC1>(o[0], rS, to); st128<SC1>(o[1], rS, to + 512);
      st64s(u32x2{st, tag}, rS, go);
      PRIO(0);
      STAMP(14);
    }
    if (!helper) {
      if (S1A < 12) BAR();
      WAIT_L0(); BAR(); CLK3(13);
      if (S1A < 12) { wait_v<1>(); ftflag(); STAMP4(1); }
      unsigned ca, cg;
      u32x4 sel, cr[2];
      f32x4 cs = z4;
      auto col_job = [&](int s, int i) {
        if (i == 7) { cr[0] = lds128(ca + s * STG); cr[1] = lds128(ca + s * STG + 8192); }
        if (i == 11) { PRIO(3); cs = mfma(cr[0], sel, cs); cs = mfma(cr[1], sel, cs); pin(cs); SB(); PRIO(0); }
      };
      stage(2, [&](int i) {
        if (i == 3) {
          ca = lds + 16384 + (rb & 2) * 8192 + (((c >> 1) & 3) + 8 * (c >> 3) + 4 * (q & 1) + 16 * (q >> 1)) * 256 + (((4 * (rb & 1) + wi) ^ (c >> 1)) << 5) + (c & 1) * 16;
#pragma unroll
          for (int e = 0; e < 4; e++) sel[e] = ((c >> 1) & 3) == e ? ((c & 1) ? 0x3f800000u : 0x3f80u) : 0u;
          pin(sel); pin(ca);
        }
        col_job(0, i);
        if (S1A == 12 && i == FTI) { wait_v<FTI + 2>(); ftflag(); STAMP4(1); }
      });
      BAR(); CLK3(14);
      stage(3, [&](int i) {
        if (i == 3) { cg = (lane & 24) ? OOB : SY_CG + cb * 8192 + (64 * (rb & 3) + 16 * wi + 8 * (lane >> 5) + c) * 32 + (rb >> 2) * 8; pin(cg); }
        col_job(1, i);
      });
      unsigned tc0 = 0, tc1 = 0;
      auto touch = [&](int s) {
        if (s == TCA) tc0 = ald32(rD, (idx * 8 + wi) * 8192 + lane * 128);
        if (s == TCB) tc1 = ald32(rD, (idx * 8 + 4 + wi) * 8192 + lane * 128);
      };
#define TN(s) ((s == TCA) + (s == TCB))
      touch(3);
      STAMP2(15); wait_v<(S1A == 12 && FTI == 11 ? 13 : 12) + TN(3)>(); BAR(); STAMP2(16); CLK3(15);
      stage(4, [&](int i) {
        if (i == 3) {
          float v = swap32_add(cs[0] + cs[2], cs[1] + cs[3]);
          v = swap16_add(v, v);
          st64s(u32x2{asu(v), tag}, rS, cg);
        }
      });
      touch(4);
      STAMP2(17); wait_v<13 + TN(4)>(); BAR(); STAMP2(18); CLK3(16);
      stage(5, none);
      dset();
      touch(5);
      STAMP2(19); wait_v<12 + TN(5)>(); BAR(); STAMP2(20); CLK3(17);
      stage(6, none);
      unsigned grv = SY_RG + (128 * rb + 32 * wi + (lane & 31)) * 8, gcv = SY_CG + cb * 8192 + (64 * wi + lane) * 32;
      pin(grv); pin(gcv);
      touch(6);
      STAMP2(21); wait_v<12 + TN(6)>(); BAR(); STAMP2(22); CLK3(18);
      u32x2 gr = ald64s(rS, grv, 0);
      u32x4 g0 = ald128s(rS, gcv, 0), g1 = ald128s(rS, gcv, 16);
      stage(7, none);
      STAMP2(23); wait_v<15>(); BAR(); STAMP2(24); CLK3(19);
      wait_v<8>(); CLK3(20); dd(0, 9); CLK3(21);
      pin(gr); pin(g0); pin(g1); pin(tc0); pin(tc1);
#pragma unroll 1
      for (int it = 0; it < SPIN; it++) {
        if (__all(gr[1] == tag && g0[1] == tag && g0[3] == tag && g1[1] == tag && g1[3] == tag)) break;
        __builtin_amdgcn_s_sleep(2);
        asm volatile("buffer_load_dwordx2 %0, %3, %5, 0 offen sc1\n\tbuffer_load_dwordx4 %1, %4, %5, 0 offen sc1\n\t"
                     "buffer_load_dwordx4 %2, %4, %5, 0 offen offset:16 sc1\n\ts_waitcnt vmcnt(0)"
                     : "=&v"(gr), "=&v"(g0), "=&v"(g1) : "v"(grv), "v"(gcv), "s"(rS) : "memory");
      }
      CLK3(22);
      const float sd = bflo(gr[0]), rr = __builtin_amdgcn_rcpf(sd);
      float s1 = -2.302208198f * rr, s2 = rr * rr, s3 = sd * sd;
      pin(s1); pin(s2); pin(s3);
      s2 *= 0.044715f; pin(s2); s2 *= s1; pin(s2);
      *(LDSP f32x4*)(lds + L_ST + (32 * wi + ((lane & 31) ^ (16 * half))) * 16) = f32x4{bfhi(gr[0]), s1, s3, s2};
      *(LDSP float*)(lds + L_CS + (64 * wi + lane) * 4) = add1(add1(asf(g0[0]), asf(g0[2])), add1(asf(g1[0]), asf(g1[2])));
      CLK3(23); wait_v<8>(); dd(1, 10); CLK3(24); wait_v<8>(); CLK3(25); dd(2, 11); CLK3(26);
      STAMP2(25); WAIT_L0(); BAR();
      STAMP(2); CLK(29);
      wait_v<THA>(); dd(3, 12); wait_v<THA>(); dd(4, 13); wait_v<THA>(); dd(5, 0);
    }
  }

  if (helper) {
    WAIT_L0(); BAR();
    STAMP(1);
    if (HP) PRIO(1);
    tag = __builtin_amdgcn_readfirstlane(*(LDSP unsigned*)(lds + L_CN + CN_T));
    {
      const unsigned g = 8 * q + wave, a0 = lds + c * 2056 + 16 * g, wo = OFF_WT + 8 * g * CP + hid * 256 + c * 16;
      u32x2 x[4][3], y[4][3];
#pragma unroll
      for (int i = 0; i < 3; i++) {
#pragma unroll
        for (int e = 0; e < (i < 2 ? 4 : 3); e++) {
          const unsigned a = a0 + 514 * e + 2 - 2 * ((e + 1) & 3) + 8 * i;
          x[e][i] = *(volatile LDSP u32x2*)a; y[e][i] = *(volatile LDSP u32x2*)(a + 4 * FRUN);
        }
      }
      if (wave >= 4) { wait_v<1>(); ftflag(); }
#pragma unroll
      for (int j = 0; j < 8; j++) {
        u32x4 w;
#pragma unroll
        for (int e = 0; e < 4; e++) { const int n = ((e + 1) & 3) + j; w[e] = __builtin_amdgcn_perm(y[e][n >> 2][(n >> 1) & 1], x[e][n >> 2][(n >> 1) & 1], (n & 1) ? 0x07060302u : 0x05040100u); }
        st128<SC1>(w, rS, wo + j * CP);
        SB();
        if (j == BJ) { WAIT_L0(); BAR(); }
        if (HAL) AL();
      }
    }
    STAMP2(15);
    if (HP) PRIO(0);
    dset();
    unsigned tb_ = lds + L_TB - 2048 + lane * 16;
    pin(tb_);
    __builtin_assume(tb_ < LDS_BYTES);
    const unsigned tb = tb_ + 2048;
    u32x4 er = lds128(tb), ei = lds128(tb + 1024), fr = lds128(tb - 2048), fi = lds128(tb - 1024);
    const unsigned col = 8 * hid + wave, slab = lds + (wave >> 1) * 16448 + (wave & 1) * 8192;
    unsigned tg = (128 * (lane >> 3 & 1) + 64 * (q & 1)) * CP + (lane >> 5) * TP + (((lane & 7) ^ (4 * (q & 1))) << 4);
    unsigned ta = slab + (q & 1) * 320 + (q >> 1) * 128 + c * 4, tc = ta + ((q & 1) ? -64 : 64), tr = lds + q * 16448 + wave * 64 + c * 4;
    unsigned wg = OFF_WT + col * CP + q * 2048 + c * 16;
    unsigned fv = SY_FT + (hid & 2) * 16384 + wave * 4096 + ftpos(lane), fw = lane < 4 ? SY_FW + (hid & 7) * 4096 + (4 * wave + lane) * 128 : OOB;
    pin(er); pin(ei); pin(fr); pin(fi); pin(tg); pin(ta); pin(tc); pin(tr); pin(wg); pin(fv); pin(fw);
    if (!HF || wave >= 4) first();
    if (wave < 4) __builtin_amdgcn_s_sleep(FDLY);
    __builtin_amdgcn_s_sleep(FDL2);
    unsigned wf = ald32s(rS, fv, 0);
    wait_v<1>();
    st32s(tag, rS, lane == 0 ? SY_FW + wave * 4096 + hid * 128 : OOB);
    STAMP4(16);
    wait_v<1>(); pin(wf);
    if (!__all(wf == tag)) poll<FNAP>(rS, fv, tag);
    STAMP4(17);
#pragma unroll
    for (int i = 0; i < 8; i++) {
      const unsigned so = OFF_T + (32 * (i & 1) + 8 * (i & 2) + (i >> 2) + 2 * wave) * CP + hid * 128 - (i & 3) * 1024;
      if (!(i & 3)) asm volatile("s_add_i32 m0, %0, %1\n\ts_nop 0" :: "s"(slab), "i"(i * 1024) : "scc");
      asm volatile("buffer_load_dwordx4 %0, %1, %2 offen offset:%3 sc1 lds" :: "v"(tg), "s"(rS), "s"(so), "i"((i & 3) * 1024) : "memory");
    }
    wf = ald32s(rS, fw, 0);
    asm volatile("s_mov_b32 m0, %0" :: "s"(slab));
    u32x4 xa[4][2], wv[4][2];
    f32x4 yr[4][2], yi[4][2];
    auto pairs = [&](f32x4 a, f32x4 b) { return u32x4{pk2(a[0], b[0]), pk2(a[1], b[1]), pk2(a[2], b[2]), pk2(a[3], b[3])}; };
    auto hold = [&]() {
#pragma unroll
      for (int i = 0; i < 8; i++) { pin(yr[i >> 1][i & 1]); pin(yi[i >> 1][i & 1]); }
      SB();
    };
    auto weave = [&]() {
      __builtin_amdgcn_sched_group_barrier(0x002, 8, 0);
#pragma unroll
      for (int i = 0; i < 16; i++) { __builtin_amdgcn_sched_group_barrier(0x008, 1, 0); __builtin_amdgcn_sched_group_barrier(0x002, 2, 0); }
      SB();
    };
    auto dh = [&](int buf, int n) {
      dma16(rD, lds + wi * 12288 + (wave >> 2) * 2048, bufaddr(buf), dv[0], step(n) * 32768 + (wave >> 2) * 16384);
      dma16n(rD, 1024, dv[1], step(n) * 32768 + (wave >> 2) * 16384);
    };
    auto wld = [&](int n) { wv[n & 3][n >> 2] = ald128s(rS, wg, (n & 3) * 512 + (n >> 2) * 256); };
#pragma unroll
    for (int i = 0; i < 4; i++) {
      const int t = i & 1;
      asm volatile("s_waitcnt vmcnt(%0)" :: "i"(7 - 2 * i) : "memory");
#pragma unroll
      for (int e = 0; e < 4; e++) { xa[0][0][e] = *(LDSP unsigned*)(ta + i * 2048 + e * 512); xa[0][1][e] = *(LDSP unsigned*)(tc + i * 2048 + e * 512); }
      if (i == 3) {
        SB();
        wait_v<0>(); pin(wf);
        if (!__all(fw == OOB || wf == tag)) poll<FNAP>(rS, fw, tag);
        STAMP4(18);
        BAR();
#pragma unroll
        for (int n = 0; n < WL0; n++) wld(n);
      }
#pragma unroll
      for (int h = 0; h < 2; h++) { yr[h][t] = mfma(xa[0][h], mr[i], t ? yr[h][0] : z4); yi[h][t] = mfma(xa[0][h], mi[i], t ? yi[h][0] : z4); }
      SB();
      if (t) {
#pragma unroll
        for (int h = 0; h < 2; h++) {
          const u32x4 u = pairs(yr[h][1], yi[h][1]), v = pairs(twice(yr[h][0], yr[h][1]), twice(yi[h][0], yi[h][1]));
#pragma unroll
          for (int r = 0; r < 4; r++) {
            asm volatile("ds_write_addtid_b32 %0 offset:%1" :: "v"(u[r]), "i"((i >> 1) * 4096 + r * 1024 + h * 256) : "memory");
            asm volatile("ds_write_addtid_b32 %0 offset:%1" :: "v"(v[r]), "i"((i >> 1) * 4096 + r * 1024 + h * 256 + 512) : "memory");
          }
          if (i == 3 && WL0 + h < WL1) wld(WL0 + h);
        }
        SB();
      }
    }
    STAMP2(19);
    WAIT_L0(); BAR();
#pragma unroll
    for (int i = 0; i < 8; i++) {
#pragma unroll
      for (int e = 0; e < 4; e++) xa[i >> 1][i & 1][e] = *(LDSP unsigned*)(tr + i * 512 + e * 4096);
      asm volatile("" ::: "memory");
    }
    SB();
#pragma unroll
    for (int r = 0; r < 4; r++) {
      yr[r][0] = mfma(er, xa[r][0], z4); yi[r][0] = mfma(ei, xa[r][0], z4); yr[r][1] = mfma(fr, xa[r][1], z4); yi[r][1] = mfma(fi, xa[r][1], z4);
      SB();
#pragma unroll
      for (int n = WL1 + (8 - WL1) * r / 4; n < WL1 + (8 - WL1) * (r + 1) / 4; n++) wld(n);
    }
    STAMP2(20);
    fr = lds128(tb + 2 * 6144); fi = lds128(tb + 2 * 6144 + 1024);
    BAR();
    if (HQ == 1 || (HQ == 2 && wave >= 4)) PRIO(1);
    STAMP2(24);
#pragma unroll
    for (int r = 0; r < 4; r++) {
      asm volatile("s_waitcnt vmcnt(%0)" :: "i"(DDP < 2 ? 6 : 6 - 2 * r) : "memory");
      if (r == 0) STAMP2(25);
      if (r == 3) STAMP2(26);
      const int t = r >> 1, e = (r & 1) * 2;
      pin(wv[e][t]); pin(wv[e + 1][t]);
      if (HAL) AL();
#pragma unroll
      for (int n = 0; n < 4; n++) {
        const f32x2 wr = {bflo(wv[e][t][n]), bflo(wv[e + 1][t][n])}, wq = {bfhi(wv[e][t][n]), bfhi(wv[e + 1][t][n])};
        const f32x2 a = {yr[n][t][e], yr[n][t][e + 1]}, b = {yi[n][t][e], yi[n][t][e + 1]}, u = b * wr + a * wq, v = a * wr - b * wq;
        yr[n][t][e] = u[0]; yr[n][t][e + 1] = u[1]; yi[n][t][e] = v[0]; yi[n][t][e + 1] = v[1];
      }
      SB();
      if (DDP < 2 && r < 3) dh(3 + r, r < 2 ? 12 + r : 0);
      if (DDP == 3 && r == 3 && wave < 4) { dd(0, 9); dd(1, 10); }
    }
    hold();
    STAMP2(27);
    f32x4 uc[2], us[2];
#pragma unroll
    for (int t = 0; t < 2; t++) { uc[t] = ldsf4(tb + 2048 + t * 2048); us[t] = ldsf4(tb + 3072 + t * 2048); }
    if (HAL) AL(); else SB();
#pragma unroll
    for (int r = 0; r < 4; r++) {
      const u32x4 u = pairs(yr[r][0], yi[r][0]), v = pairs(yr[r][1], yi[r][1]);
      yr[r][1] = mfma(u, er, z4); yi[r][1] = mfma(u, ei, z4);
      yr[r][0] = mfma(v, fr, yr[r][1]); yi[r][0] = mfma(v, fi, yi[r][1]);
    }
    weave();
    STAMP2(21);
    u32x4 gr[2], gi[2];
#pragma unroll
    for (int r = 0; r < 4; r++) {
      f32x4 nc[2], ns[2];
#pragma unroll
      for (int t = 0; t < 2; t++) if (r < 3) { nc[t] = ldsf4(tb + (r + 1) * 6144 + 2048 + t * 2048); ns[t] = ldsf4(tb + (r + 1) * 6144 + 3072 + t * 2048); }
      if (r & 1) { gr[r >> 1] = lds128(tb + r * 6144); gi[r >> 1] = lds128(tb + r * 6144 + 1024); }
      if (HAL) { pin(uc[1]); pin(us[1]); AL(); } else SB();
      yr[r][1] = twice(yr[r][1], yr[r][0]); yi[r][1] = twice(yi[r][1], yi[r][0]);
#pragma unroll
      for (int t = 0; t < 2; t++) {
        const f32x4 a = yr[r][t];
        yr[r][t] = a * uc[t] + yi[r][t] * us[t]; yi[r][t] = yi[r][t] * uc[t] - a * us[t];
        uc[t] = nc[t]; us[t] = ns[t];
      }
      SB();
      if (DDP == 1 && r < 3) dh(r, 9 + r);
      if (DDP == 3 && wave < 4) dd(2 + r, (11 + r) % 14);
    }
    hold();
    if (HAL) { pin(gr[1]); pin(gi[1]); AL(); }
#pragma unroll
    for (int t = 0; t < 2; t++)
#pragma unroll
      for (int n = 0; n < 4; n++) {
        const int r = (2 - n) & 3;
        const u32x4 v = pairs(yr[r][t], yi[r][t]);
        yr[r][t] = mfma(r == 0 ? er : r == 2 ? fr : gr[r >> 1], v, n < 2 ? z4 : yr[r ^ 2][t]);
        yi[r][t] = mfma(r == 0 ? ei : r == 2 ? fi : gi[r >> 1], v, n < 2 ? z4 : yi[r ^ 2][t]);
      }
    weave();
    STAMP2(22);
    unsigned zo = OFF_Z + col * CP + q * 2048 + c * 16;
    float sg = (hid & 8) ? 1.f : -1.f, ng = -sg;
    pin(sg); pin(ng); pin(zo);
#pragma unroll
    for (int t = 0; t < 2; t++) {
      if (HAL) AL();
      const f32x4 ur = twice(yr[2][t], yr[0][t]), ui = twice(yi[2][t], yi[0][t]), dr = twice(yi[1][t], yi[3][t]), di = twice(yr[1][t], yr[3][t]);
      const f32x4 r0 = yr[0][t] + yr[3][t], i0 = yi[0][t] + yi[3][t], r2 = twice(yr[3][t], r0), i2 = twice(yi[3][t], i0);
      const f32x4 r1 = ur + ng * dr, i1 = ui + sg * di, r3 = ur + sg * dr, i3 = ui + ng * di;
#pragma unroll
      for (int e = 0; e < 4; e++)
        st128<SC1>(u32x4{pk2(r0[e], i0[e]), pk2(r1[e], i1[e]), pk2(r2[e], i2[e]), pk2(r3[e], i3[e])}, rS, zo + (e >> 1) * 1024 + t * 512 + (e & 1) * 256);
      SB();
    }
    STAMP4(23);
    if (wave < 4) {
      wait_v<8>(); lds_set(cML, 4);
      STAMP(2); CLK(29);
      done(threadIdx.x ? OOB : SY_FZ + hid * 128, true);
    } else {
      lds_bits(cML, 4);
      STAMP(2); CLK(29);
#pragma unroll
      for (int i = 0; i < (HG ? 16 : 4 * GV + 4); i++) asm volatile("" : "=v"(G[i & 3][i >> 2]));
      asm volatile("" : "=v"(fh), "=v"(fl));
    }
#pragma unroll
    for (int p = 0; p < 4; p++) asm volatile("" : "=v"(own[p]));
  }

  unsigned ob, rw, ra[4];
  auto rset = [&](int y) {
    if (y == 4) { rw = lds + wi * 12288 + lane * 16; pin(rw); __builtin_assume(rw < 65536); }
    if (y < 4) { ra[y] = lds + (wi ^ y) * 12288 + bufaddr(y == 0 ? 1 : y == 1 ? 6 : 6 - y) + (wave >> 2) * 2048 + lane * 16; pin(ra[y]); __builtin_assume(ra[y] < LDS_BYTES); }
  };
  int w3 = wave;
  asm volatile("" : "+s"(w3));
  if (w3 >= 4) {
    u32x4 Bq[7][4];
    unsigned fz = (lane >> 3) == 1 ? SY_FZ + (8 * wi - 8 + lane) * 128 : OOB;
    unsigned fv = lane < 7 ? SY_FH + (rb * 4 + wi) * 2048 + (7 - 7 * half + lane) * 128 : ZL ? OOB : fz;
    if (ZL) pin(fz);
    unsigned hv[4], tb[2][4], or0;
#pragma unroll
    for (int p = 0; p < 4; p++) {
      hv[p] = hbase(p); tb[0][p] = tbase() ^ (p << 5); tb[1][p] = tb[0][p] + 2 * STG;
      pin(hv[p]); pin(tb[0][p]); pin(tb[1][p]); __builtin_assume(tb[0][p] < LDS_BYTES); __builtin_assume(tb[1][p] < LDS_BYTES);
    }
    ob = obase(); or0 = ob - 64;
    pin(fv); pin(ob); pin(or0);
    auto ldb = [&](int j, int buf) {
#pragma unroll
      for (int v = 0; v < 4; v++) Bq[j][v] = ldstr2<2048>(tb[buf < 3][v] + bufaddr(buf) % (2 * STG));
    };
    if (!helper) PRIO(LPA);
    wait_v<12>();
#pragma unroll
    for (int j = 0; j < 3; j++) ldb(j, j);
    WAIT_L0();
#pragma unroll
    for (int j = 0; j < 12; j++) pin(Bq[j >> 2][j & 3]);
    auto pace = [&](int b, auto n_) {
      if (!helper) { if (b == GATE) { wait_v<0>(); lds_set(cLM, 1); lds_wait4(lds + L_CN + CN_S); if (GP) PRIO(1); lds_bits(cML, 2); PRIO(LPG); } }
      else if (b == HBP) { wait_v<4 * (HBP < 2 ? 2 : HBP) + 2>(); BAR(); PRIO(LPH); }
    };
    dd(0, 1); dd(1, 2);
    u32x4 res[2] = {ald128(rX, or0), ald128(rX, ob)};
    if (!helper) wait_v<10>(); else pace(1, Cn<0>{});
#pragma unroll
    for (int j = 3; j < 6; j++) ldb(j, j);
    WAIT_L0();
#pragma unroll
    for (int j = 12; j < 24; j++) pin(Bq[j >> 2][j & 3]);
    pace(2, Cn<THB>{});
    unsigned fe;
    if (EL) { fe = helper ? fv : OOB; asm volatile("buffer_load_dword %0, %0, %1, 0 offen sc1" : "+v"(fe) : "s"(rS) : "memory"); }
    dd(2, 3);
    pace(3, Cn<THR2>{}); dd(3, 4); pace(4, Cn<THR2>{}); dd(4, 5);
    STAMP(4);
    dd(6, 6); dd(7, 7);
    unsigned fl[10], st;
    auto post = [&](unsigned& v, unsigned w, unsigned z = 4) {
      pin(v);
      fv = v == tag ? OOB : fv;
      const unsigned m = (unsigned)__builtin_amdgcn_ballot_w64(fv != OOB);
      st = w | ((m & 0xff00) ? 0 : z) | ((m & 0xff) ? 0 : 8);
      lds_set(cLM, st);
    };
    auto until = [&](unsigned m) {
#pragma unroll 1
      for (int it = 0; it < SPIN && !(st & m); it++) {
        __builtin_amdgcn_s_sleep(ZNAP);
        unsigned v = ald32s(rS, fv, 0);
        wait_v<0>();
        post(v, 19, ZL ? m & 4 : 4);
      }
    };
    unsigned f1 = ald32s(rS, fv, 0);
    dd(8, 8);
    STAMP(5);
    wait_v<13>();
    if (EL) { pin(fe); fe = helper ? fe : ~tag; post(fe, 17, 0); } else lds_set(cLM, 17);
    if (!EL || !(st & 8)) {
      wait_v<4>();
      unsigned f2;
      asm volatile("v_mov_b32 %0, %1" : "=v"(f2) : "v"(f1));
      post(f2, 17, ZL ? 0 : 4);
      until(8);
    }
    if (ZL) fv = fz;
    STAMP(6);
    PRIO(LPR);
    STAMP(8);
    {
      u32x4 A[28];
      auto jn = [](int n) { return n < 16 ? n >> 2 : 4 + (n - 16) % 3; };
      auto pn = [](int n) { return n < 16 ? n & 3 : (n - 16) / 3; };
      auto la = [&](int n) { if (n < 28) A[n] = ald128s(rS, hv[pn(n)], step(jn(n) < 6 ? (9 + jn(n)) % 14 : 8) * 4096); };
      unsigned f0;
      if (ES) f0 = ald32s(rS, fv, 0);
#pragma unroll
      for (int n = 0; n < LW; n++) la(n);
      SB();
      f32x4 a2[4][4];
      unsigned rx[4];
      pin(res[0]); pin(res[1]);
#pragma unroll
      for (int i = 0; i < 8; i++) {
        const unsigned a = res[i >> 2][(i >> 1) & 1], b = res[i >> 2][2 + ((i >> 1) & 1)];
        const auto r = __builtin_amdgcn_permlane16_swap((i & 1) ? a & 0xffff0000u : a << 16, (i & 1) ? b & 0xffff0000u : b << 16, false, false);
        a2[3][(i >> 2) * 2][i & 3] = asf(r[0]); a2[3][(i >> 2) * 2 + 1][i & 3] = asf(r[1]);
      }
#pragma unroll
      for (int v = 0; v < 4; v++) pin(a2[3][v]);
      SB();
      sfor<0, 28>(EACH(n) {
        constexpr int n = n_.v;
        wait_v<younger(n)>();
        pin(A[n]);
        if ((n & 1) && n > 4) STAMP5(13 + n / 2);
        if (n == 8) ldb(6, 8);
        if (n == 0) { pin(f1); if (ES) post(f0, 19); else lds_set(cLM, st | 2); }
        if constexpr (n > LW && (n - LW - 1) % 3 == 0) post(fl[(n - LW - 1) / 3], 19);
        la(n + LW);
        if (n % 3 == 0 && n + LW < 27) fl[n / 3] = ald32s(rS, fv, 0);
        if (n == 12) {
#pragma unroll
          for (int y = 0; y < 5; y++) rset(y);
#pragma unroll
          for (int y = 0; y < 4; y++) { rx[y] = lds + (wi ^ y) * 12288 + bufaddr(y == 3 ? 5 : y == 2 ? 8 : 0) + lane * 16; pin(rx[y]); __builtin_assume(rx[y] < LDS_BYTES); }
        }
        if (n == 25) lds_bits(cML, 1);
        SB();
#pragma unroll
        for (int v = 0; v < 4; v++) {
          constexpr int p = pn(n), t = 4 * ((n - 16) % 3);
          a2[p][v] = mfma(Bq[jn(n)][v], A[n], n < 3 ? z4 : a2[p][v]);
          SB();
          if (n > 18 && (t + v) % 3 == 2) *(LDSP f32x4*)((p == 3 ? rx[0] : rw + bufaddr(p == 1 ? 5 : 8)) + (t + v) / 3 * 1024) = a2[(p - 1) & 3][(t + v) / 3];
          SB();
        }
      });
      lds_set(lds + L_CN + CN_R + wi * 4, 1);
      until(4);
      PRIO(LP2);
      STAMP(7);
      if (bid == 0 && wave == 4) st32s(tag, rS, lane == 0 ? SY_E : OOB);
      lds_wait4(lds + L_CN + CN_R);
#pragma unroll
      for (int y = 1; y < 4; y++)
#pragma unroll
        for (int v = 0; v < 4; v++) a2[y - 1][v] = ldsf4(rx[y] + v * 1024);
      SB();
#pragma unroll
      for (int y = 0; y < 3; y++)
#pragma unroll
        for (int v = 0; v < 4; v++) {
          if (PKR) a2[3][v] += a2[y][v];
          else {
#pragma unroll
            for (int e = 0; e < 4; e++) a2[3][v][e] = add1(a2[3][v][e], a2[y][v][e]);
          }
          SB();
        }
#pragma unroll
      for (int v = 0; v < 2; v++) *(LDSP f32x4*)(ra[0] - 2048 + v * 1024) = a2[3][v];
      o0 = a2[3][2]; o1 = a2[3][3];
    }
    STAMP(9); CLK(30);
  } else {
    f32x4 a2[4][4];
    u32x4 A[44], B[8][4], zf[2];
    unsigned hv[4], tb[4], so[8];
    unsigned zv = OFF_Z + 16 * q * CP + rb * 512 + (16 * half + c) * 16;
    constexpr int Z0 = 4 * ZS, T0 = Z0 + 16, NT = 7 - ZS;
    auto la = [&](int m) { A[m] = m < Z0 ? ald128s(rS, hv[m & 3], so[1 + (m >> 2)]) : m < T0 ? ald128s(rS, zv, (64 * wi + m - Z0) * CP) : ald128s(rS, hv[(m - T0) / NT], so[ZS + 1 + (m - T0) % NT]); };
    auto lb = [&](int j, int i) {
      const u32x2 t = ldstr(tb[i >> 1] + (j < 4 ? j - 1 : j < 6 ? j - 4 : j + 6) * 4096 + (i & 1) * 2048);
      B[j][i >> 1][(i & 1) * 2] = t[0]; B[j][i >> 1][(i & 1) * 2 + 1] = t[1];
    };
    auto pick = [&](int m, int e) { zf[m & 1][e] = A[(m & 60) + e][m & 3]; zf[m & 1][e + 1] = A[(m & 60) + e + 1][m & 3]; pin(zf[m & 1]); };
    if (OWN) {
      swp<0>(wi, own[0], own[1]); swp<0>(wi, own[2], own[3]);
      swp<1>(wi, own[0], own[2]); swp<1>(wi, own[1], own[3]);
    }
    if (!HG || !helper) {
#pragma unroll
      for (int v = GV + 1; v < 4; v++) gen(v);
    }
#pragma unroll
    for (int p = 0; p < 4; p++) { hv[p] = hbase(p); tb[p] = (tbase() ^ (p << 5)) + 2 * STG; pin(hv[p]); pin(tb[p]); __builtin_assume(tb[p] < LDS_BYTES); }
#pragma unroll
    for (int j = 1; j < 8; j++) { so[j] = step(j) * 4096; asm volatile("" : "+s"(so[j])); }
    pin(zv);
    if (EM && helper) { if (EM == 2) wait_v<0>(); pin(fl); fh = fl == tag ? OOB : fh; }
    if (!EM || !__all(fh == OOB)) {
      __builtin_amdgcn_s_sleep(P1);
      PRIO(3);
#pragma unroll 1
      for (int it = 0; it < SPIN; it++) {
        unsigned v = ald32s(rS, fh, 0);
        wait_v<0>(); pin(v);
        fh = v == tag ? OOB : fh;
        if (__all(fh == OOB)) break;
        __builtin_amdgcn_s_sleep(HNAP);
      }
    }
    STAMP(5);
    if (MP) PRIO(MP - 1);
    if (OWN) {
      if (helper) {
#pragma unroll
        for (int m = 0; m < 4; m++) asm volatile("buffer_load_dwordx4 %0, %1, %2, %3 offen sc1" : "+v"(own[m]) : "v"(hv[m]), "s"(rS), "s"(so[1]) : "memory");
      }
#pragma unroll
      for (int m = 0; m < 4; m++) A[m] = own[m];
    }
#pragma unroll
    for (int m = OWN ? 4 : 0; m < 12; m++) la(m);
    lds_bits(cLM, 1);
    STAMP(6);
#pragma unroll
    for (int i = 0; i < 8; i++) lb(1, i);
    wait_v<11>(); pin(A[0]);
    SB();
    sfor<0, 44>(EACH(m) {
      constexpr int m = m_.v, u = m >> 2, p = m < T0 ? m & 3 : (m - T0) / NT, j = m < T0 ? u + 1 : ZS + 1 + (m - T0) % NT;
      constexpr bool z = m >= Z0 && m < T0;
      constexpr int CZ = Z0 - 12, CW = ZS == 6 ? 16 : T0 - 8;
      if ((m & 3) == 3) STAMP5(13 + u);
      if (m == 3 || m == CZ - 1 || m == CW - 2) fl = *(volatile LDSP unsigned*)cLM;
      if (m == 4 || m == CZ || m == CW) {
        constexpr unsigned k = (m == 4 ? 16 : 0) | (m == CZ ? 4 : 0) | (m == CW ? 2 : 0);
        if ((__builtin_amdgcn_readfirstlane(fl) & k) != k) lds_bits(cLM, k);
        if (m == CZ) STAMP(7);
      }
      if (m == 32) STAMP(8);
      if constexpr ((m & 3) == 3 && m > Z0 - 2 && m < T0 - 4) {
        wait_v<(m < 33 ? 7 : 39 - m)>();
#pragma unroll
        for (int i = 1; i < 5; i++) pin(A[m + i]);
      }
      SB();
#pragma unroll
      for (int v = 0; v < 4; v++) {
        const int w = m == 43 ? v ^ 2 : v;
        a2[p][w] = z ? mfma(G[(u - ZS) & 3][w], zf[m & 1], a2[p][w]) : mfma(B[j & 7][w], A[m], m > 3 ? a2[p][w] : z4);
        SB();
        if (u < ZS - 1 && v < 3 && 3 * p + v < 8) lb(j + 1, 3 * p + v);
        if (z && u + 4 - ZS > ZS && v == 2) { lb(u + 4 - ZS, 2 * p); lb(u + 4 - ZS, 2 * p + 1); }
        if (m == 7) { tb[v] -= 2 * STG; pin(tb[v]); __builtin_assume(tb[v] < LDS_BYTES); }
        if (m == 2 && v == 2) lds_set(cML, 7);
        if (v < 2 && m > Z0 - 2 && m < T0 - 1) pick(m + 1, 2 * v);
        if constexpr (m < Z0 - 1 || (m > T0 - 2 && m < 43)) if (v == 2) { wait_v<(m < 33 ? 10 : 42 - m)>(); pin(A[m + 1]); }
        if (v == 3 && m < 32) la(m + 12);
        if (v == 0 && m == 32) { ob = obase(); pin(ob); }
        if (m == 33 || m == 34) rset(4 * (m - 33) + v);
        constexpr int t = 4 * ((m + 8 * NT - T0) % NT);
        if (m >= T0 + NT && (t + v) % NT == NT - 1) *(LDSP f32x4*)(rw + bufaddr(p == 3 ? 6 : 2 + p) + (t + v) / NT * 1024) = a2[(p - 1) & 3][(t + v) / NT];
        SB();
      }
    });
#pragma unroll
    for (int v = 2; v < 4; v++) *(LDSP f32x4*)(ra[0] + v * 1024) = a2[3][v];
    o0 = a2[3][0]; o1 = a2[3][1];
    STAMP(9);
  }
  WAIT_L0();
  STAMP(10);
  BAR();
  STAMP(11);
  {
    f32x4 r[4][2];
#pragma unroll
    for (int y = 0; y < 4; y++) { r[y][0] = ldsf4(ra[y]); r[y][1] = ldsf4(ra[y] + 1024); }
    SB();
#pragma unroll
    for (int y = 0; y < 4; y++) { o0 += r[y][0]; o1 += r[y][1]; }
    float lo[4], hi[4];
#pragma unroll
    for (int e = 0; e < 4; e++) { const auto r = __builtin_amdgcn_permlane16_swap(asu(o0[e]), asu(o1[e]), false, false); lo[e] = asf(r[0]); hi[e] = asf(r[1]); }
    st128<OAUX>(u32x4{pk2(lo[0], lo[1]), pk2(lo[2], lo[3]), pk2(hi[0], hi[1]), pk2(hi[2], hi[3])}, mk_rsrc(pout, X_BYTES), ob);
  }
  STAMP(12);
  FLUSH();
}

extern "C" void kernel_launch(void* const* d_in, const int* in_sizes, int n_in, void* d_out, int out_size, void* d_ws, size_t ws_size,
                              hipStream_t stream) {
  (void)in_sizes; (void)n_in; (void)out_size; (void)ws_size;
  (void)hipFuncSetAttribute((const void*)fused, hipFuncAttributeMaxDynamicSharedMemorySize, LDS_BYTES);
  fused<<<256, 512, LDS_BYTES, stream>>>(d_in[0], d_in[1], d_in[2], d_in[4], d_in[5], d_in[6], (char*)d_ws, d_out);
}
```
